```python
import torch

def _softmax(x, axis=-1):
    return torch.softmax(x, dim=axis)
def _gelu(x):
    return torch.nn.functional.gelu(x, approximate="tanh")
def _silu(x):
    return torch.nn.functional.silu(x)
def _sigmoid(x):
    return torch.sigmoid(x)
def _layernorm(x, axis=-1, eps=1e-5):
    m = x.mean(dim=axis, keepdim=True)
    v = x.var(dim=axis, keepdim=True, unbiased=False)
    return (x - m) / torch.sqrt(v + eps)
def _scatter_add(tgt, idx, src, axis=0):
    return tgt.clone().index_add_(axis, idx, src)
def _segment_sum(src, seg, n):
    out = torch.zeros((n,) + src.shape[1:], dtype=src.dtype)
    return out.index_add_(0, seg, src)
def _pad(x, pw):
    flat = tuple(v for pair in reversed(pw) for v in pair)
    return torch.nn.functional.pad(x, flat)

kernel_name = 'gated_mix__mha_block__glu_gated_bilinear__prompt'

def setup_inputs():

    _w = (lambda a: (lambda t: t.to(torch.bfloat16) if t.is_floating_point() else t)(torch.as_tensor(a)))
    def _A_setup():
        B, S, D, H = 1, 2048, 512, 32
        return {
            "x": _w((lambda *s: torch.randn(*s, dtype=torch.bfloat16) * 0.05)(B, S, D)),
            "wqkv": _w((lambda *s: torch.randn(*s, dtype=torch.bfloat16) * 0.8)(D, 3 * D)),
            "wo": _w((lambda *s: torch.randn(*s, dtype=torch.bfloat16) * 0.05)(D, D)),
        }
    _A_in = _A_setup()

    def _B_setup():
        B, S, D, F = 1, 2048, 512, 1792
        return {
            "x": _w((lambda *s: torch.randn(*s, dtype=torch.bfloat16) * 0.05)(B, S, D)),
            "w_gate": _w((lambda *s: torch.randn(*s, dtype=torch.bfloat16) * 1.6)(D, F) * (D ** -0.5)),
            "w_up": _w((lambda *s: torch.randn(*s, dtype=torch.bfloat16) * 1.6)(D, F) * (D ** -0.5)),
            "w_down": _w((lambda *s: torch.randn(*s, dtype=torch.bfloat16) * 1.6)(F, D) * (F ** -0.5)),
        }
    _B_in = _B_setup()

    return {
        "A_x": _A_in["x"],
        "A_wqkv": _A_in["wqkv"],
        "A_wo": _A_in["wo"],
        "B_x": _B_in["x"],
        "B_w_gate": _B_in["w_gate"],
        "B_w_up": _B_in["w_up"],
        "B_w_down": _B_in["w_down"],
        "gate_w": _w((lambda *s: torch.randn(*s, dtype=torch.bfloat16) * 0.05)(512) * 16.0),
    }

def reference(A_x, A_wqkv, A_wo, B_x, B_w_gate, B_w_up, B_w_down, gate_w):
    def _A_ref(x, wqkv, wo):
        B, S, D = x.shape
        H = 32
        Dh = D // H
        qkv = (lambda x, *d: x.permute(*d))((x @ wqkv).reshape(B, S, 3, H, Dh), 2, 0, 3, 1, 4)
        q, k, v = qkv[0], qkv[1], qkv[2]
        scores = torch.einsum("bhqd,bhkd->bhqk", q, k) * (Dh ** -0.5)
        p = _softmax(scores, axis=-1)
        o = torch.einsum("bhqk,bhkd->bhqd", p, v)
        return (lambda x, *d: x.permute(*d))(o, 0, 2, 1, 3).reshape(B, S, D) @ wo

    def _B_ref(x, w_gate, w_up, w_down):
        g = torch.einsum("bsd,df->bsf", x, w_gate)
        u = torch.einsum("bsd,df->bsf", x, w_up)
        h = g * u
        return torch.einsum("bsf,fd->bsd", h, w_down)

    x = A_x
    g = _sigmoid((x * gate_w).sum(-1, keepdims=True))
    a = _A_ref(x, A_wqkv, A_wo)
    b = _B_ref(x, B_w_gate, B_w_up, B_w_down)
    return g * a + (1 - g) * b
```

```cpp
#include <hip/hip_runtime.h>
#include <hip/hip_bf16.h>
#include <stdint.h>

typedef short bf16x8 __attribute__((ext_vector_type(8)));
typedef short s16x4 __attribute__((ext_vector_type(4)));
typedef float f32x16 __attribute__((ext_vector_type(16)));
typedef float f32x4 __attribute__((ext_vector_type(4)));
typedef unsigned u32x4 __attribute__((ext_vector_type(4)));
typedef unsigned short u16;
#define LAS __attribute__((address_space(3)))
#define DI __device__ __forceinline__
#define MFMA32(a, b, c) __builtin_amdgcn_mfma_f32_32x32x16_bf16((a), (b), (c), 0, 0, 0)
#define SBAR() __builtin_amdgcn_sched_barrier(0)
#define PIN(x) asm volatile("" : "+v"(x))
template <int V> struct IC { static constexpr int value = V; };
template <int I, int N, class F> DI void static_for(F&& f) { if constexpr (I < N) { f(IC<I>{}); static_for<I + 1, N>(f); } }

constexpr int SEQ = 2048, DM = 512, NH = 32, FF = 1792;
constexpr int NTT = SEQ / 32;
constexpr int KSD = FF / 16;
constexpr int KSO = DM / 16;
constexpr size_t WS_QKV = 0;
constexpr size_t WS_H = WS_QKV + (size_t)96 * NTT * 1024;
constexpr size_t WS_O = WS_H + (size_t)NTT * KSD * 1024;
constexpr size_t WS_WD = WS_O + (size_t)NTT * KSO * 1024;
constexpr size_t WS_WO = WS_WD + (size_t)16 * KSD * 1024;
constexpr size_t WS_B = WS_WO + (size_t)16 * KSO * 1024;
constexpr size_t WS_G = WS_B + (size_t)SEQ * DM * 2;
constexpr float QSCALE = 0.25f * 1.4426950408889634f;

typedef float f32x2 __attribute__((ext_vector_type(2)));
typedef __bf16 bf16x2_t __attribute__((ext_vector_type(2)));
DI unsigned cvtpk(float lo, float hi) { const f32x2 v = {lo, hi}; return __builtin_bit_cast(unsigned, __builtin_convertvector(v, bf16x2_t)); }
DI float bf2f(u16 v) { return __uint_as_float((unsigned)v << 16); }
DI s16x4 tr_read(LAS const char* p) { return __builtin_amdgcn_ds_read_tr16_b64_v4i16((LAS s16x4*)p); }
DI bf16x8 cat8(s16x4 a, s16x4 b) { return __builtin_shufflevector(a, b, 0, 1, 2, 3, 4, 5, 6, 7); }
DI void dma16(unsigned voff, const void* sbase, unsigned lds) {
    asm volatile("s_mov_b32 m0, %2\n\ts_nop 0\n\tglobal_load_lds_dwordx4 %0, %1" :: "v"(voff), "s"(sbase), "s"(lds) : "memory");
}
#define STAMP(k, i) do { } while (0)
#define WAIT_VM_BAR(N) asm volatile("s_waitcnt vmcnt(" #N ") lgkmcnt(0)\n\ts_barrier" ::: "memory")

constexpr int K1_NSLOT = 20, K1_LDS = K1_NSLOT * 8192;
template <int N> DI void wait_vm_bar() {
    if constexpr (N == 13) WAIT_VM_BAR(13);
    else if constexpr (N == 6) WAIT_VM_BAR(6);
    else if constexpr (N == 0) WAIT_VM_BAR(0);
    else static_assert(N == 0, "add the count");
}

__global__ __launch_bounds__(512, 2) void k1_proj(const u16* __restrict__ x, const u16* __restrict__ wqkv, const u16* __restrict__ wg,
                                                   const u16* __restrict__ wu, const u16* __restrict__ wd, char* __restrict__ ws) {
    extern __shared__ __attribute__((aligned(16))) char lds[];
    const int tid = threadIdx.x, lane = tid & 63, wid = __builtin_amdgcn_readfirstlane(tid >> 6);
    const int r32 = lane & 31, hh = lane >> 5;
    const int wm = wid & 3, wc = wid >> 2;
    const int xcd = blockIdx.x & 7, bi = blockIdx.x >> 3;
    const int rb = (xcd & 1) * 8 + (bi & 7);
    const int cb = (xcd >> 1) * 4 + (bi >> 3);
    const bool typeB = (cb & 3) == 3;
    const int ia = cb - (cb >> 2), ib = cb >> 2;
    const int fu0 = typeB ? 48 + 2 * ib : 4 * ia;
    const int qu0 = typeB ? 24 + 6 * ib : 2 * ia;

    STAMP(0, 0);
    u16 rp[8];
    const int rpf = blockIdx.x * 7 + (wid < 6 ? wid : 6);
    {
        const int nt = rpf / KSD, ks = rpf - nt * KSD;
        const u16* src = wd + (size_t)(ks * 16 + 4 * hh) * DM + nt * 32 + r32;
#pragma unroll
        for (int j = 0; j < 8; ++j) rp[j] = src[(8 * (j >> 2) + (j & 3)) * DM];
    }

    const int prow = 8 * wid + (lane >> 3), pch = lane & 7;
    const unsigned xoff = (unsigned)(prow * 1024 + ((pch ^ ((prow >> 1) & 7)) << 4));
    const unsigned wsw = (unsigned)((pch ^ (((prow >> 1) & 1) << 2)) << 4);
    const unsigned woffF = (unsigned)(prow * (FF * 2)) + wsw, woffQ = (unsigned)(prow * (3 * DM * 2)) + wsw;
    const char* xb = (const char*)x + (size_t)rb * 128 * 1024;
    const char* b0 = (const char*)wg + fu0 * 64;
    const char* b1 = (const char*)wu + fu0 * 64;
    const char* b2 = typeB ? (const char*)wqkv + (qu0 + 2) * 64 : (const char*)wg + (fu0 + 2) * 64;
    const char* b3 = typeB ? (const char*)wqkv + (qu0 + 4) * 64 : (const char*)wu + (fu0 + 2) * 64;
    const char* b4 = (const char*)wqkv + qu0 * 64;
    const unsigned woff23 = typeB ? woffQ : woffF;
    const size_t stepF = (size_t)64 * FF * 2, stepQ = (size_t)64 * 3 * DM * 2, step23 = typeB ? stepQ : stepF;
    const unsigned lds0 = (unsigned)(uintptr_t)lds + wid * 1024;
    auto issue = [&](auto P) {
        constexpr int p = decltype(P)::value, s = p / 7, i = p % 7;
        if constexpr (p < 56) {
            const unsigned dst = lds0 + (p % K1_NSLOT) * 8192;
            if constexpr (i == 0) dma16(xoff, xb + s * 128, dst);
            else if constexpr (i == 1) dma16(xoff, xb + 65536 + s * 128, dst);
            else if constexpr (i == 2) dma16(woffF, b0 + s * stepF, dst);
            else if constexpr (i == 3) dma16(woffF, b1 + s * stepF, dst);
            else if constexpr (i == 4) dma16(woff23, b2 + s * step23, dst);
            else if constexpr (i == 5) dma16(woff23, b3 + s * step23, dst);
            else dma16(woffQ, b4 + s * stepQ, dst);
        }
    };
    static_for<0, K1_NSLOT>(issue);

    LAS const char* L = (LAS const char*)lds;
    const int xr = (wm & 1) * 32 + r32;
    int xrd[4];
#pragma unroll
    for (int ks = 0; ks < 4; ++ks) xrd[ks] = (wm >> 1) * 8192 + xr * 128 + (((2 * ks + hh) ^ ((xr >> 1) & 7)) << 4);
    const int q4 = (lane & 15) >> 2;
    int wb[3];
    wb[0] = (8 * hh + q4) * 128 + ((wc ^ (q4 >> 1)) << 6) + ((lane >> 4) & 1) * 32 + (lane & 3) * 8;
    wb[1] = wb[0] + 65536; wb[2] = wb[0] + 131072;
    PIN(wb[0]); PIN(wb[1]); PIN(wb[2]);

    f32x16 acc[5];
#pragma unroll
    for (int u = 0; u < 5; ++u)
#pragma unroll
        for (int i = 0; i < 16; ++i) acc[u][i] = 0.f;
    bf16x8 xf[2], wf[2][5];
    auto rd_x = [&](int n) { return *(LAS const bf16x8*)(L + ((7 * (n >> 2)) % K1_NSLOT) * 8192 + xrd[n & 3]); };
    auto rd_w = [&](int n, int u) {
        const int off = ((7 * (n >> 2) + 2 + u) % K1_NSLOT) * 8192 + (n & 3) * 2048;
        return cat8(tr_read(L + wb[off >> 16] + (off & 65535)), tr_read(L + wb[off >> 16] + (off & 65535) + 512));
    };
    wait_vm_bar<13>();
    STAMP(0, 1);
    xf[0] = rd_x(0);
#pragma unroll
    for (int u = 0; u < 5; ++u) wf[0][u] = rd_w(0, u);
    static_for<0, 32>([&](auto N) {
        constexpr int n = decltype(N)::value, cu = n & 1, nx = cu ^ 1;
        constexpr bool seam = (n & 3) == 3 && n < 31;
        constexpr int p0 = K1_NSLOT + 7 * (n >> 2);
        if constexpr (seam) { if constexpr (n == 27) wait_vm_bar<0>(); else wait_vm_bar<6>(); }
        static_for<0, 5>([&](auto U) {
            constexpr int u = decltype(U)::value;
            acc[u] = MFMA32(wf[cu][u], xf[cu], acc[u]);
            SBAR();
            if constexpr (n < 31) { if constexpr (u == 0) xf[nx] = rd_x(n + 1); wf[nx][u] = rd_w(n + 1, u); }
            if constexpr (seam && u < 4) { issue(IC<p0 + 2 * u>{}); if constexpr (u < 3) issue(IC<p0 + 2 * u + 1>{}); }
            SBAR();
        });
    });
    STAMP(0, 2);

    const int tt = rb * 4 + wm;
    auto store_h = [&](const f32x16& a, const f32x16& b, int funit) {
#pragma unroll
        for (int s = 0; s < 2; ++s) {
            u32x4 v;
#pragma unroll
            for (int j = 0; j < 4; ++j) v[j] = cvtpk(a[8 * s + 2 * j] * b[8 * s + 2 * j], a[8 * s + 2 * j + 1] * b[8 * s + 2 * j + 1]);
            *(u32x4*)(ws + WS_H + ((size_t)tt * KSD + 2 * funit + s) * 1024 + lane * 16) = v;
        }
    };
    auto store_qkv = [&](const f32x16& a, int qunit) {
        const float sc = qunit < 16 ? QSCALE : 1.f;
        const int pos = qunit >= 32 ? (hh * 512 + ((r32 ^ (hh << 2)) << 4)) : lane * 16;
#pragma unroll
        for (int s = 0; s < 2; ++s) {
            u32x4 v;
#pragma unroll
            for (int j = 0; j < 4; ++j) v[j] = cvtpk(a[8 * s + 2 * j] * sc, a[8 * s + 2 * j + 1] * sc);
            *(u32x4*)(ws + WS_QKV + ((size_t)(2 * qunit + s) * NTT + tt) * 1024 + pos) = v;
        }
    };
    store_h(acc[0], acc[1], fu0 + wc);
    if (typeB) { store_qkv(acc[2], qu0 + 2 + wc); store_qkv(acc[3], qu0 + 4 + wc); }
    else store_h(acc[2], acc[3], fu0 + 2 + wc);
    store_qkv(acc[4], qu0 + wc);

    {
        u32x4 w;
#pragma unroll
        for (int j = 0; j < 4; ++j) w[j] = (unsigned)rp[2 * j] | ((unsigned)rp[2 * j + 1] << 16);
        *(u32x4*)(ws + WS_WD + (size_t)rpf * 1024 + lane * 16) = w;
    }
    STAMP(0, 3);
}

constexpr int AT_ONES = 65536, AT_LDS = 65536 + 8192;

DI f32x16 attn_exact(const char* __restrict__ kg, const char* __restrict__ vg, bf16x8 qf, int lane) {
    const int r32 = lane & 31, hh = lane >> 5;
    f32x16 o, zero;
#pragma unroll
    for (int i = 0; i < 16; ++i) { o[i] = 0.f; zero[i] = 0.f; }
    float m = -INFINITY;
#pragma unroll 1
    for (int t = 0; t < NTT; ++t) {
        const bf16x8 kf = *(const bf16x8*)(kg + t * 1024 + lane * 16);
        f32x16 s = MFMA32(kf, qf, zero);
        float tm = s[0];
#pragma unroll
        for (int i = 1; i < 16; ++i) tm = fmaxf(tm, s[i]);
        tm = fmaxf(tm, __shfl_xor(tm, 32));
        const float mn = fmaxf(m, tm);
        const float alpha = __builtin_amdgcn_exp2f(m - mn);
        m = mn;
#pragma unroll
        for (int i = 0; i < 16; ++i) o[i] *= alpha;
#pragma unroll
        for (int st = 0; st < 2; ++st) {
            u32x4 pw; bf16x8 vf;
#pragma unroll
            for (int j = 0; j < 4; ++j) pw[j] = cvtpk(__builtin_amdgcn_exp2f(s[8 * st + 2 * j] - m), __builtin_amdgcn_exp2f(s[8 * st + 2 * j + 1] - m));
#pragma unroll
            for (int j = 0; j < 8; ++j) {
                const int key = 16 * st + 8 * (j >> 2) + 4 * hh + (j & 3), d = r32 & 15, dh = (d >> 2) & 1, dj = (d >> 3) * 4 + (d & 3);
                const u16 val = *(const u16*)(vg + t * 1024 + dh * 512 + ((key ^ (dh << 2)) << 4) + dj * 2);
                vf[j] = r32 < 16 ? (short)val : (short)0x3f80;
            }
            o = MFMA32(vf, __builtin_bit_cast(bf16x8, pw), o);
        }
    }
    return o;
}

DI void attn_body(int blk, char* __restrict__ ws, char* lds) {
    const int tid = threadIdx.x, lane = tid & 63, wid = __builtin_amdgcn_readfirstlane(tid >> 6);
    const int r32 = lane & 31, hh = lane >> 5;
    const int xcd = blk & 7, bi = blk >> 3;
    const int head = xcd * 4 + (bi >> 3), qb = bi & 7;
    const char* qg = ws + WS_QKV + ((size_t)head * NTT + qb * 8 + wid) * 1024;
    const char* kg = ws + WS_QKV + (size_t)(32 + head) * NTT * 1024;
    const char* vg = ws + WS_QKV + (size_t)(64 + head) * NTT * 1024;
    const unsigned lds0 = (unsigned)(uintptr_t)lds + wid * 1024;
    const unsigned doff = (unsigned)(tid * 16);
    STAMP(1, 0);
    const bf16x8 qf = *(const bf16x8*)(qg + lane * 16);
    auto issue = [&](int c) { dma16(doff, kg + c * 8192, lds0 + (c & 3) * 16384); dma16(doff, vg + c * 8192, lds0 + (c & 3) * 16384 + 8192); };
    issue(0); issue(1); issue(2);
    { const unsigned o1 = ((tid & 1) == 0) ? 0x00003f80u : 0u; const u32x4 ones = {o1, 0u, 0u, 0u}; *(u32x4*)(lds + AT_ONES + tid * 16) = ones; }
    LAS const char* L = (LAS const char*)lds;
    const int q4 = (lane & 15) >> 2, p4 = lane & 3, dh = p4 & 1;
    const bool isdata = ((lane >> 4) & 1) == 0;
    const int vlane = dh * 512 + (((4 * hh + q4) ^ (dh << 2)) << 4) + (p4 >> 1) * 8;
    const int vone = AT_ONES + 128 + (lane & 15) * 8;
    WAIT_VM_BAR(2);

    STAMP(1, 1);
    f32x16 negm, o;
    {
        f32x16 zero;
#pragma unroll
        for (int i = 0; i < 16; ++i) { zero[i] = 0.f; o[i] = 0.f; }
        const bf16x8 kf = *(LAS const bf16x8*)(L + lane * 16);
        const f32x16 s = MFMA32(kf, qf, zero);
        float tm = s[0];
#pragma unroll
        for (int i = 1; i < 16; ++i) tm = fmaxf(tm, s[i]);
        tm = fmaxf(tm, __shfl_xor(tm, 32));
#pragma unroll
        for (int i = 0; i < 16; ++i) negm[i] = -tm;
    }
    f32x16 sA = MFMA32(*(LAS const bf16x8*)(L + lane * 16), qf, negm), sB;
    bf16x8 kf = *(LAS const bf16x8*)(L + lane * 16 + 1024);
    const int vb0 = isdata ? 8192 + vlane : vone;
    bf16x8 v0 = cat8(tr_read(L + vb0), tr_read(L + vb0 + 128)), v1 = v0;
    u32x4 p0, p1 = {0u, 0u, 0u, 0u};
#define EX2(X, i) X[i] = __builtin_amdgcn_exp2f(X[i])
#define TILE(SC, SN, KNEXT, V1ADDR, V0NEXT) do { \
        o = MFMA32(v1, __builtin_bit_cast(bf16x8, p1), o); SBAR(); \
        v1 = cat8(tr_read(L + (V1ADDR)), tr_read(L + (V1ADDR) + 128)); \
        EX2(SC, 0); EX2(SC, 1); EX2(SC, 2); EX2(SC, 3); EX2(SC, 4); EX2(SC, 5); PIN(SC); SBAR(); \
        SN = MFMA32(kf, qf, negm); SBAR(); \
        kf = *(LAS const bf16x8*)(L + (KNEXT)); \
        EX2(SC, 6); EX2(SC, 7); p0[0] = cvtpk(SC[0], SC[1]); p0[1] = cvtpk(SC[2], SC[3]); p0[2] = cvtpk(SC[4], SC[5]); p0[3] = cvtpk(SC[6], SC[7]); PIN(p0); \
        EX2(SC, 8); EX2(SC, 9); EX2(SC, 10); PIN(SC); SBAR(); \
        o = MFMA32(v0, __builtin_bit_cast(bf16x8, p0), o); SBAR(); \
        v0 = cat8(tr_read(L + (V0NEXT)), tr_read(L + (V0NEXT) + 128)); \
        EX2(SC, 11); EX2(SC, 12); EX2(SC, 13); EX2(SC, 14); EX2(SC, 15); \
        p1[0] = cvtpk(SC[8], SC[9]); p1[1] = cvtpk(SC[10], SC[11]); p1[2] = cvtpk(SC[12], SC[13]); p1[3] = cvtpk(SC[14], SC[15]); PIN(p1); SBAR(); \
    } while (0)
    for (int c = 0; c < 8; ++c) {
        const int kb = (c & 3) * 16384 + lane * 16, kbn = ((c + 1) & 3) * 16384 + lane * 16;
        const int vb = isdata ? (c & 3) * 16384 + 8192 + vlane : vone;
        const int vbn = isdata ? ((c + 1) & 3) * 16384 + 8192 + vlane : vone;
        TILE(sA, sB, kb + 2048, vb + 256, vb + 1024);
        TILE(sB, sA, kb + 3072, vb + 1024 + 256, vb + 2048);
        if (c == 0) { }
        else if (c < 6) { WAIT_VM_BAR(2); }
        else { WAIT_VM_BAR(0); }
        if (c + 3 < 8) issue(c + 3);
        TILE(sA, sB, kb + 4096, vb + 2048 + 256, vb + 3072);
        TILE(sB, sA, kb + 5120, vb + 3072 + 256, vb + 4096);
        TILE(sA, sB, kb + 6144, vb + 4096 + 256, vb + 5120);
        TILE(sB, sA, kb + 7168, vb + 5120 + 256, vb + 6144);
        TILE(sA, sB, kbn, vb + 6144 + 256, vb + 7168);
        TILE(sB, sA, kbn + 1024, vb + 7168 + 256, vbn);
    }
    o = MFMA32(v1, __builtin_bit_cast(bf16x8, p1), o);
    STAMP(1, 2);
    o[8] = __shfl(o[8], r32);
    {
        bool ok = o[8] < 1.8446744e19f;
#pragma unroll
        for (int i = 0; i < 8; ++i) ok = ok && (fabsf(o[i]) < 3.0e38f);
        if (__builtin_expect(!__all(ok), 0)) o = attn_exact(kg, vg, qf, lane);
    }
    const float inv = 1.f / o[8];
    u32x4 w;
#pragma unroll
    for (int j = 0; j < 4; ++j) w[j] = cvtpk(o[2 * j] * inv, o[2 * j + 1] * inv);
    *(u32x4*)(ws + WS_O + ((size_t)(qb * 8 + wid) * KSO + head) * 1024 + lane * 16) = w;
    STAMP(1, 3);
}

constexpr int RED_BLK = 1024 + 64;

template <int NKS, int DEPTH>
DI void frag_gemm(const char* __restrict__ a0, const char* __restrict__ a1, const char* __restrict__ w0, const char* __restrict__ w1, f32x16 (&acc)[4]) {
    bf16x8 fa0[DEPTH], fa1[DEPTH], fw0[DEPTH], fw1[DEPTH];
#pragma unroll
    for (int i = 0; i < DEPTH; ++i) {
        fa0[i] = *(const bf16x8*)(a0 + i * 1024); fw0[i] = *(const bf16x8*)(w0 + i * 1024);
        fa1[i] = *(const bf16x8*)(a1 + i * 1024); fw1[i] = *(const bf16x8*)(w1 + i * 1024);
    }
#pragma unroll
    for (int k = 0; k < NKS; ++k) {
        const int i = k % DEPTH;
        acc[0] = MFMA32(fw0[i], fa0[i], acc[0]); acc[1] = MFMA32(fw1[i], fa0[i], acc[1]);
        acc[2] = MFMA32(fw0[i], fa1[i], acc[2]); acc[3] = MFMA32(fw1[i], fa1[i], acc[3]);
        if (k + DEPTH < NKS) {
            fa0[i] = *(const bf16x8*)(a0 + (k + DEPTH) * 1024); fw0[i] = *(const bf16x8*)(w0 + (k + DEPTH) * 1024);
            fa1[i] = *(const bf16x8*)(a1 + (k + DEPTH) * 1024); fw1[i] = *(const bf16x8*)(w1 + (k + DEPTH) * 1024);
        }
        SBAR();
    }
}
DI void red_write(char* lds, int wid, int lane, const f32x16& a, const f32x16& b) {
#pragma unroll
    for (int q = 0; q < 4; ++q) {
        *(f32x4*)(lds + ((wid * 2 + 0) * 4 + q) * RED_BLK + lane * 16) = (f32x4){a[4 * q], a[4 * q + 1], a[4 * q + 2], a[4 * q + 3]};
        *(f32x4*)(lds + ((wid * 2 + 1) * 4 + q) * RED_BLK + lane * 16) = (f32x4){b[4 * q], b[4 * q + 1], b[4 * q + 2], b[4 * q + 3]};
    }
}
DI f32x4 red_read(const char* lds, int tid) {
    const int tok = tid >> 4, cg = tid & 15;
    const char* p = lds + (cg >> 1) * RED_BLK + ((cg & 1) * 32 + tok) * 16;
    f32x4 s = *(const f32x4*)p;
#pragma unroll
    for (int w = 1; w < 8; ++w) s += *(const f32x4*)(p + w * 8 * RED_BLK);
    return s;
}
constexpr int RED_LDS = 64 * RED_BLK;

DI void down_body(int blk, const u16* __restrict__ x, const u16* __restrict__ gw, const u16* __restrict__ wo, char* __restrict__ ws, char* lds) {
    const int tid = threadIdx.x, lane = tid & 63, wid = __builtin_amdgcn_readfirstlane(tid >> 6);
    const int xcd = blk & 7, bi = blk >> 3;
    const int mb = xcd * 4 + (bi >> 3), nb = bi & 7;
    STAMP(2, 0);
    if (wid < 2) {
        const int f = blk * 2 + wid, nt = f >> 5, ks = f & 31;
        const u16* src = wo + (size_t)(ks * 16 + 4 * (lane >> 5)) * DM + nt * 32 + (lane & 31);
        u32x4 w;
#pragma unroll
        for (int j = 0; j < 4; ++j) w[j] = (unsigned)src[(8 * (j >> 1) + 2 * (j & 1)) * DM] | ((unsigned)src[(8 * (j >> 1) + 2 * (j & 1) + 1) * DM] << 16);
        *(u32x4*)(ws + WS_WO + (size_t)f * 1024 + lane * 16) = w;
    }
    {
        const int tok = blk * 8 + wid;
        const u32x4 xv = *(const u32x4*)(x + (size_t)tok * DM + lane * 8), gv = *(const u32x4*)(gw + lane * 8);
        float s = 0.f;
#pragma unroll
        for (int j = 0; j < 4; ++j) {
            s += __uint_as_float(xv[j] << 16) * __uint_as_float(gv[j] << 16);
            s += __uint_as_float(xv[j] & 0xffff0000u) * __uint_as_float(gv[j] & 0xffff0000u);
        }
#pragma unroll
        for (int o = 32; o >= 1; o >>= 1) s += __shfl_xor(s, o);
        if (lane == 0) ((float*)(ws + WS_G))[tok] = 1.f / (1.f + __expf(-s));
    }
    f32x16 acc[4];
#pragma unroll
    for (int u = 0; u < 4; ++u)
#pragma unroll
        for (int i = 0; i < 16; ++i) acc[u][i] = 0.f;
    const char* a0 = ws + WS_H + ((size_t)(2 * mb) * KSD + wid * 14) * 1024 + lane * 16;
    const char* w0 = ws + WS_WD + ((size_t)(2 * nb) * KSD + wid * 14) * 1024 + lane * 16;
    STAMP(2, 1);
    frag_gemm<14, 2>(a0, a0 + KSD * 1024, w0, w0 + KSD * 1024, acc);
    STAMP(2, 2);
    u16* bout = (u16*)(ws + WS_B);
#pragma unroll
    for (int mt = 0; mt < 2; ++mt) {
        if (mt) __syncthreads();
        red_write(lds, wid, lane, acc[2 * mt], acc[2 * mt + 1]);
        __syncthreads();
        const f32x4 s = red_read(lds, tid);
        uint2 w; w.x = cvtpk(s[0], s[1]); w.y = cvtpk(s[2], s[3]);
        *(uint2*)(bout + (size_t)(mb * 64 + mt * 32 + (tid >> 4)) * DM + nb * 64 + (tid & 15) * 4) = w;
    }
    STAMP(2, 3);
}

constexpr int K2_LDS = AT_LDS > RED_LDS ? AT_LDS : RED_LDS;
__global__ __launch_bounds__(512, 4) void k2_mid(const u16* __restrict__ x, const u16* __restrict__ gw, const u16* __restrict__ wo, char* __restrict__ ws) {
    extern __shared__ __attribute__((aligned(16))) char lds[];
    if (blockIdx.x < 256) attn_body(blockIdx.x, ws, lds);
    else down_body(blockIdx.x - 256, x, gw, wo, ws, lds);
}

__global__ __launch_bounds__(512, 2) void k3_out(char* __restrict__ ws, u16* __restrict__ out) {
    extern __shared__ __attribute__((aligned(16))) char lds[];
    const int tid = threadIdx.x, lane = tid & 63, wid = __builtin_amdgcn_readfirstlane(tid >> 6);
    const int xcd = blockIdx.x & 7, bi = blockIdx.x >> 3;
    const int mb = xcd * 4 + (bi >> 3), nb = bi & 7;
    f32x16 acc[4];
#pragma unroll
    for (int u = 0; u < 4; ++u)
#pragma unroll
        for (int i = 0; i < 16; ++i) acc[u][i] = 0.f;
    const char* a0 = ws + WS_O + ((size_t)(2 * mb) * KSO + wid * 4) * 1024 + lane * 16;
    const char* w0 = ws + WS_WO + ((size_t)(2 * nb) * KSO + wid * 4) * 1024 + lane * 16;
    const u16* bin = (const u16*)(ws + WS_B);
    STAMP(3, 0);
    uint2 bv[2]; float g[2];
#pragma unroll
    for (int mt = 0; mt < 2; ++mt) {
        const int tok = mb * 64 + mt * 32 + (tid >> 4);
        bv[mt] = *(const uint2*)(bin + (size_t)tok * DM + nb * 64 + (tid & 15) * 4);
        g[mt] = ((const float*)(ws + WS_G))[tok];
    }
    frag_gemm<4, 4>(a0, a0 + KSO * 1024, w0, w0 + KSO * 1024, acc);
    STAMP(3, 1);
    red_write(lds, wid, lane, acc[0], acc[1]);
    red_write(lds + RED_LDS, wid, lane, acc[2], acc[3]);
    __syncthreads();
    STAMP(3, 2);
#pragma unroll
    for (int mt = 0; mt < 2; ++mt) {
        const f32x4 s = red_read(lds + mt * RED_LDS, tid);
        const float ga = g[mt], gb = 1.f - ga;
        const float o0 = ga * s[0] + gb * __uint_as_float(bv[mt].x << 16), o1 = ga * s[1] + gb * __uint_as_float(bv[mt].x & 0xffff0000u);
        const float o2 = ga * s[2] + gb * __uint_as_float(bv[mt].y << 16), o3 = ga * s[3] + gb * __uint_as_float(bv[mt].y & 0xffff0000u);
        uint2 w; w.x = cvtpk(o0, o1); w.y = cvtpk(o2, o3);
        *(uint2*)(out + (size_t)(mb * 64 + mt * 32 + (tid >> 4)) * DM + nb * 64 + (tid & 15) * 4) = w;
    }
    STAMP(3, 3);
}

extern "C" void kernel_launch(void* const* d_in, const int* in_sizes, int n_in, void* d_out, int out_size, void* d_ws, size_t ws_size,
                              hipStream_t stream) {
    const u16* x = (const u16*)d_in[0];
    const u16* wqkv = (const u16*)d_in[1];
    const u16* wo = (const u16*)d_in[2];
    const u16* wg = (const u16*)d_in[4];
    const u16* wu = (const u16*)d_in[5];
    const u16* wd = (const u16*)d_in[6];
    const u16* gw = (const u16*)d_in[7];
    char* ws = (char*)d_ws;
    (void)hipFuncSetAttribute((const void*)k1_proj, hipFuncAttributeMaxDynamicSharedMemorySize, K1_LDS);
    (void)hipFuncSetAttribute((const void*)k2_mid, hipFuncAttributeMaxDynamicSharedMemorySize, K2_LDS);
    (void)hipFuncSetAttribute((const void*)k3_out, hipFuncAttributeMaxDynamicSharedMemorySize, 2 * RED_LDS);
    k1_proj<<<256, 512, K1_LDS, stream>>>(x, wqkv, wg, wu, wd, ws);
    k2_mid<<<512, 512, K2_LDS, stream>>>(x, gw, wo, ws);
    k3_out<<<256, 512, 2 * RED_LDS, stream>>>(ws, (u16*)d_out);
}
```

```cpp
#include <hip/hip_runtime.h>
#include <hip/hip_bf16.h>
#include <stdint.h>

typedef short bf16x8 __attribute__((ext_vector_type(8)));
typedef short s16x4 __attribute__((ext_vector_type(4)));
typedef float f32x16 __attribute__((ext_vector_type(16)));
typedef float f32x4 __attribute__((ext_vector_type(4)));
typedef float f32x2 __attribute__((ext_vector_type(2)));
typedef unsigned u32x4 __attribute__((ext_vector_type(4)));
typedef unsigned u32x2 __attribute__((ext_vector_type(2)));
typedef __bf16 bf16x2_t __attribute__((ext_vector_type(2)));
typedef unsigned short u16;
typedef unsigned long long u64;
#define LAS __attribute__((address_space(3)))
#define GAS __attribute__((address_space(1)))
#define DI __device__ __forceinline__
#define MFMA32(a, b, c) __builtin_amdgcn_mfma_f32_32x32x16_bf16((a), (b), (c), 0, 0, 0)
#define MFMA16(a, b, c) __builtin_amdgcn_mfma_f32_16x16x32_bf16((a), (b), (c), 0, 0, 0)
#define SBAR() __builtin_amdgcn_sched_barrier(0)
#define PIN(x) asm volatile("" : "+v"(x))
#define BC8(x) __builtin_bit_cast(bf16x8, x)
template <int V> struct IC { static constexpr int value = V; };
template <int I, int N, class F> DI void static_for(F&& f) { if constexpr (I < N) { f(IC<I>{}); static_for<I + 1, N>(f); } }

constexpr int SEQ = 2048, DM = 512, FF = 1792;
constexpr unsigned WS_E = 0;
constexpr unsigned WS_KVF = 4096;
constexpr unsigned WS_HF = WS_KVF + 8192;
constexpr unsigned WS_OF = WS_HF + 16384;
constexpr unsigned WS_KV = 65536;
constexpr unsigned WS_H = WS_KV + (4u << 20);
constexpr unsigned WS_O = WS_H + 128u * 56 * 1024;
constexpr unsigned WS_END = WS_O + 128u * 16 * 1024;
constexpr float QSCALE = 0.25f * 1.4426950408889634f;
constexpr int LDS_BYTES = 81920;

DI unsigned cvtpk(float lo, float hi) { const f32x2 v = {lo, hi}; return __builtin_bit_cast(unsigned, __builtin_convertvector(v, bf16x2_t)); }
DI s16x4 tr_read(LAS const char* p) { return __builtin_amdgcn_ds_read_tr16_b64_v4i16((LAS s16x4*)p); }
DI bf16x8 cat8(s16x4 a, s16x4 b) { return __builtin_shufflevector(a, b, 0, 1, 2, 3, 4, 5, 6, 7); }
DI void dma16(unsigned voff, const void* sbase, unsigned lds) {
    asm volatile("s_mov_b32 m0, %2\n\ts_nop 0\n\tglobal_load_lds_dwordx4 %0, %1" :: "v"(voff), "s"(sbase), "s"(lds) : "memory");
}
DI void dma16_dev(unsigned voff, const void* sbase, unsigned lds) {
    asm volatile("s_mov_b32 m0, %2\n\ts_nop 0\n\tglobal_load_lds_dwordx4 %0, %1 sc1" :: "v"(voff), "s"(sbase), "s"(lds) : "memory");
}
#define WAIT_VM(N) asm volatile("s_waitcnt vmcnt(" #N ")" ::: "memory")
#define WAIT_VM_BAR(N) asm volatile("s_waitcnt vmcnt(" #N ") lgkmcnt(0)\n\ts_barrier" ::: "memory")
#define LGKM_BAR() asm volatile("s_waitcnt lgkmcnt(0)\n\ts_barrier" ::: "memory")
#define BAR() asm volatile("s_barrier" ::: "memory")
typedef __amdgpu_buffer_rsrc_t rsrc_t;
DI void st16_dev(rsrc_t r, unsigned off, u32x4 v) { __builtin_amdgcn_raw_buffer_store_b128(v, r, off, 0, 16); }
DI u32x4 ld16_dev(rsrc_t r, unsigned voff, unsigned soff) { return __builtin_amdgcn_raw_buffer_load_b128(r, voff, soff, 16); }
DI void flag_set(char* ws, unsigned off, u64 tag) { __hip_atomic_store((GAS u64*)(ws + off), tag, __ATOMIC_RELAXED, __HIP_MEMORY_SCOPE_AGENT); }
DI void flag_wait(const char* ws, unsigned off, u64 tag) {
    const u64 t0 = __builtin_amdgcn_s_memrealtime();
    for (;;) {
        const u64 v = __hip_atomic_load((const GAS u64*)(ws + off), __ATOMIC_RELAXED, __HIP_MEMORY_SCOPE_AGENT);
        if (__all(v == tag)) break;
        if (__builtin_amdgcn_s_memrealtime() - t0 > 2000000ull) break;
        __builtin_amdgcn_s_sleep(2);
    }
    asm volatile("" ::: "memory");
}
DI u64 read_tag(const char* ws) {
    u64 e;
    asm volatile("s_load_dwordx2 %0, %1, 0x0\n\ts_waitcnt lgkmcnt(0)" : "=s"(e) : "s"(ws) : "memory");
    return e * 6364136223846793005ull + 1442695040888963407ull;
}

DI int fresh_lane() { int l; asm volatile("v_mbcnt_lo_u32_b32 %0, -1, 0\n\tv_mbcnt_hi_u32_b32 %0, -1, %0" : "=v"(l)); return l; }
#define LANE16() const int lane = fresh_lane(), r16 = lane & 15, g4 = lane >> 4, q4 = r16 >> 2, p4 = lane & 3; (void)r16; (void)g4; (void)q4; (void)p4

#define STAMP(k, i) do { } while (0)

constexpr int A_NS = 19;
constexpr int A_SLOT = 17408;
constexpr int A_Q = 4 * A_SLOT;

DI f32x16 attn_exact(rsrc_t rs, unsigned kvbase, bf16x8 qf, int lane) {
    const int r32 = lane & 31, hh = lane >> 5;
    f32x16 o, zero;
#pragma unroll
    for (int i = 0; i < 16; ++i) { o[i] = 0.f; zero[i] = 0.f; }
    float m = -INFINITY;
    const unsigned cst = ((r32 & 27) == 16) ? 0x3f803f80u : 0u;
    const u32x4 cv = {cst, cst, cst, cst};
#pragma unroll 1
    for (int t = 0; t < 64; ++t) {
        const unsigned tb = kvbase + (t >> 2) * 16384 + ((t >> 1) & 1) * 4096;
        const bf16x8 kf = BC8(ld16_dev(rs, tb + (t & 1) * 1024 + lane * 16, 0));
        const f32x16 s = MFMA32(kf, qf, zero);
        float tm = s[0];
#pragma unroll
        for (int i = 1; i < 16; ++i) tm = fmaxf(tm, s[i]);
        tm = fmaxf(tm, __shfl_xor(tm, 32));
        const float mn = fmaxf(m, tm);
        const float alpha = __builtin_amdgcn_exp2f(m - mn);
        m = mn;
#pragma unroll
        for (int i = 0; i < 16; ++i) o[i] *= alpha;
#pragma unroll
        for (int st = 0; st < 2; ++st) {
            u32x4 pw;
#pragma unroll
            for (int j = 0; j < 4; ++j) pw[j] = cvtpk(__builtin_amdgcn_exp2f(s[8 * st + 2 * j] - m), __builtin_amdgcn_exp2f(s[8 * st + 2 * j + 1] - m));
            const u32x4 vv = ld16_dev(rs, tb + 8192 + hh * 1024 + ((t & 1) * 2 + st) * 256 + (r32 & 15) * 16, 0);
            o = MFMA32(BC8(r32 < 16 ? vv : cv), BC8(pw), o);
        }
    }
    return o;
}

DI void role_a(int blk, const u16* __restrict__ x, const u16* __restrict__ wqkv, char* __restrict__ ws, char* lds) {
    const int tid = threadIdx.x, lane = tid & 63, wid = __builtin_amdgcn_readfirstlane(tid >> 6);
    const int r32 = lane & 31, hh = lane >> 5;
    const int xcd = blk & 7, bi = blk >> 3;
    const int qb = (xcd & 1) * 8 + (bi & 7), hp = (xcd >> 1) * 4 + (bi >> 3);
    const unsigned lds0 = (unsigned)(uintptr_t)lds;
    LAS char* L = (LAS char*)lds;
    const rsrc_t rs = __builtin_amdgcn_make_buffer_rsrc(ws, 0, WS_END, 0x00020000);
    const unsigned kvown = WS_KV + (unsigned)(hp * 16 + qb) * 16384;
    u64 tag;
    STAMP(0, 0);
    if (wid >= 6) {
        const int ld = wid - 6;
        unsigned xo[2];
#pragma unroll
        for (int e = 0; e < 2; ++e) { const int row = 8 * (2 * ld + e) + (lane >> 3); xo[e] = (unsigned)(row * 1024 + (((lane & 7) ^ ((row >> 1) & 7)) << 4)); }
        const unsigned wo_ = (unsigned)((32 * ld + (lane >> 2)) * 3072 + (lane & 3) * 16);
        const char* xb = (const char*)x + (size_t)qb * 128 * 1024;
        const char* wb = (const char*)wqkv + hp * 64;
        unsigned dst = lds0 + ld * 2048;
        const unsigned dend = lds0 + A_NS * 4096;
        auto piece = [&](int s, auto I) {
            constexpr int i = decltype(I)::value;
            if constexpr (i < 4) { const char* b = xb + i * 32768 + s * 128; dma16(xo[0], b, dst); dma16(xo[1], b, dst + 1024); }
            else { const char* b = wb + (size_t)s * (64 * 3072) + (i - 4) * 1024; dma16(wo_, b, dst); dma16(wo_, b + 16 * 3072, dst + 1024); }
            dst += 4096; if (dst >= dend) dst -= A_NS * 4096;
        };
        static_for<0, 7>([&](auto I) { piece(0, I); });
        static_for<0, 7>([&](auto I) { piece(1, I); });
        static_for<0, 5>([&](auto I) { piece(2, I); });
        tag = read_tag(ws);
#pragma unroll 1
        for (int s = 0; s < 8; ++s) {
            if (s < 6) WAIT_VM(24); else if (s == 6) WAIT_VM(14); else WAIT_VM(0);
            BAR();
            BAR();
            if (s + 2 < 8) static_for<5, 7>([&](auto I) { piece(s + 2, I); });
            if (s + 3 < 8) static_for<0, 5>([&](auto I) { piece(s + 3, I); });
        }
        const unsigned cst = ((lane & 11) == 0) ? 0x3f803f80u : 0u;
        const u32x4 cv = {cst, cst, cst, cst};
        *(LAS u32x4*)(L + (2 * ld) * A_SLOT + 16384 + lane * 16) = cv;
        *(LAS u32x4*)(L + (2 * ld + 1) * A_SLOT + 16384 + lane * 16) = cv;
    } else {
        const int tg = wid & 1, unit = wid >> 1;
        const int xrd = r32 * 128 + ((hh ^ ((r32 >> 1) & 7)) << 4);
        const int wrd = (8 * hh + ((lane & 15) >> 2)) * 64 + ((lane >> 4) & 1) * 32 + (lane & 3) * 8;
        int px0 = 2 * tg, px1 = 2 * tg + 1, pw = 4 + unit;
        f32x16 acc[2];
#pragma unroll
        for (int i = 0; i < 16; ++i) { acc[0][i] = 0.f; acc[1][i] = 0.f; }
        tag = read_tag(ws);
#pragma unroll 1
        for (int s = 0; s < 8; ++s) {
            bf16x8 xf[2][4], wf[4];
            BAR();
            const int a0 = px0 * 4096 + xrd, a1 = px1 * 4096 + xrd, aw = pw * 4096 + wrd;
#pragma unroll
            for (int ks = 0; ks < 4; ++ks) {
                wf[ks] = cat8(tr_read(L + aw + ks * 1024), tr_read(L + aw + ks * 1024 + 256));
                xf[0][ks] = *(LAS const bf16x8*)(L + (a0 ^ (ks << 5)));
                xf[1][ks] = *(LAS const bf16x8*)(L + (a1 ^ (ks << 5)));
            }
            LGKM_BAR();
            if (unit == 2) {
#pragma unroll
                for (int ks = 0; ks < 4; ++ks) { acc[0] = MFMA32(xf[0][ks], wf[ks], acc[0]); acc[1] = MFMA32(xf[1][ks], wf[ks], acc[1]); }
            } else {
#pragma unroll
                for (int ks = 0; ks < 4; ++ks) { acc[0] = MFMA32(wf[ks], xf[0][ks], acc[0]); acc[1] = MFMA32(wf[ks], xf[1][ks], acc[1]); }
            }
            px0 += 7; if (px0 >= A_NS) px0 -= A_NS;
            px1 += 7; if (px1 >= A_NS) px1 -= A_NS;
            pw += 7; if (pw >= A_NS) pw -= A_NS;
        }
        const float sc = unit == 0 ? QSCALE : 1.f;
#pragma unroll
        for (int i = 0; i < 2; ++i)
#pragma unroll
            for (int e = 0; e < 2; ++e) {
                u32x4 v;
#pragma unroll
                for (int j = 0; j < 4; ++j) v[j] = cvtpk(acc[i][8 * e + 2 * j] * sc, acc[i][8 * e + 2 * j + 1] * sc);
                if (unit == 0) *(LAS u32x4*)(L + A_Q + ((2 * tg + i) * 2 + e) * 1024 + lane * 16) = v;
                else {
                    const int off = unit == 1 ? tg * 4096 + e * 2048 + i * 1024 + lane * 16
                                              : 8192 + tg * 4096 + (r32 >> 4) * 2048 + hh * 1024 + (2 * i + e) * 256 + (r32 & 15) * 16;
                    st16_dev(rs, kvown + off, v);
                    *(LAS u32x4*)(L + off) = v;
                }
            }
    }
    LGKM_BAR();
    STAMP(0, 1);
    const int hsel = wid & 1, qp = (wid >> 1) & 1, kh = wid >> 2;
    const bf16x8 qA = *(LAS const bf16x8*)(L + A_Q + ((2 * qp) * 2 + hsel) * 1024 + lane * 16);
    const bf16x8 qB = *(LAS const bf16x8*)(L + A_Q + ((2 * qp + 1) * 2 + hsel) * 1024 + lane * 16);
    int ka = kh * 4096 + hsel * 2048 + lane * 16;
    int va = r32 < 16 ? 8192 + kh * 4096 + hsel * 2048 + hh * 1024 + r32 * 16 : 16384 + (r32 & 15) * 16;
    PIN(ka); PIN(va);
#define LD8(A) (*(LAS const bf16x8*)(L + (A)))
    bf16x8 kf = LD8(ka);
    f32x16 negm, oA, oB, sX, sY;
    float mref;
    {
        f32x16 zero;
#pragma unroll
        for (int i = 0; i < 16; ++i) { zero[i] = 0.f; oA[i] = 0.f; oB[i] = 0.f; }
        sX = MFMA32(kf, qA, zero); sY = MFMA32(kf, qB, zero);
        float tm = fmaxf(sX[0], sY[0]);
#pragma unroll
        for (int i = 1; i < 16; ++i) tm = fmaxf(tm, fmaxf(sX[i], sY[i]));
        mref = fmaxf(tm, __shfl_xor(tm, 32));
#pragma unroll
        for (int i = 0; i < 16; ++i) negm[i] = -mref;
        PIN(negm);
    }
    sX = MFMA32(kf, qA, negm);
    bf16x8 v0 = LD8(va), v1 = v0;
    u32x4 p0, p1 = {0u, 0u, 0u, 0u};
    if (wid >= 2 && wid < 6) {
        WAIT_VM(0);
        if (lane == 0) flag_set(ws, WS_KVF + (unsigned)((hp * 4 + (wid - 2)) * 16 + qb) * 8, tag);
    }
    const char* kvg = ws + WS_KV + (size_t)hp * 16 * 16384 + wid * 2048;
    const unsigned doff = (unsigned)(lane * 16);
    auto fetch = [&](int c, int slot) {
        const char* b = kvg + (size_t)((qb + c) & 15) * 16384;
        const unsigned d = lds0 + slot * A_SLOT + wid * 2048;
        dma16_dev(doff, b, d); dma16_dev(doff, b + 1024, d + 1024);
    };
#define EX2(X, i) X[i] = __builtin_amdgcn_exp2f(X[i])
#define HALF(SC, M1, L1, M2, L2, M3, L3) do { \
        EX2(SC, 0); EX2(SC, 1); PIN(SC); SBAR(); \
        M1; SBAR(); L1; \
        EX2(SC, 2); EX2(SC, 3); EX2(SC, 4); EX2(SC, 5); EX2(SC, 6); EX2(SC, 7); PIN(SC); SBAR(); \
        M2; SBAR(); L2; \
        p0[0] = cvtpk(SC[0], SC[1]); p0[1] = cvtpk(SC[2], SC[3]); p0[2] = cvtpk(SC[4], SC[5]); p0[3] = cvtpk(SC[6], SC[7]); PIN(p0); SBAR(); \
        EX2(SC, 8); EX2(SC, 9); EX2(SC, 10); EX2(SC, 11); PIN(SC); SBAR(); \
        M3; SBAR(); L3; \
        EX2(SC, 12); EX2(SC, 13); EX2(SC, 14); EX2(SC, 15); \
        p1[0] = cvtpk(SC[8], SC[9]); p1[1] = cvtpk(SC[10], SC[11]); p1[2] = cvtpk(SC[12], SC[13]); p1[3] = cvtpk(SC[14], SC[15]); PIN(p1); SBAR(); \
    } while (0)
#define CHUNK(S0, S1, SYNC) do { \
        HALF(sX, oB = MFMA32(v1, BC8(p1), oB), v1 = LD8(va + (S0) + 256), sY = MFMA32(kf, qB, negm), kf = LD8(ka + (S0) + 1024), oA = MFMA32(v0, BC8(p0), oA), (void)0); \
        HALF(sY, oA = MFMA32(v1, BC8(p1), oA), (void)0, sX = MFMA32(kf, qA, negm), (void)0, oB = MFMA32(v0, BC8(p0), oB), v0 = LD8(va + (S0) + 512)); \
        HALF(sX, oB = MFMA32(v1, BC8(p1), oB), v1 = LD8(va + (S0) + 768), sY = MFMA32(kf, qB, negm), SYNC; kf = LD8(ka + (S1)), oA = MFMA32(v0, BC8(p0), oA), (void)0); \
        HALF(sY, oA = MFMA32(v1, BC8(p1), oA), (void)0, sX = MFMA32(kf, qA, negm), (void)0, oB = MFMA32(v0, BC8(p0), oB), v0 = LD8(va + (S1))); \
    } while (0)
#pragma unroll 1
    for (int n = 0; n < 4; ++n) {
        auto sync_a = [&]() {
            if (n == 0) {
                STAMP(0, 2);
                flag_wait(ws, WS_KVF + (unsigned)((hp * 4 + (wid >> 1)) * 16 + (lane & 15)) * 8, tag);
                STAMP(0, 3);
                fetch(1, 1); fetch(2, 2);
            }
            WAIT_VM_BAR(0);
            if (n == 0) STAMP(0, 4);
            fetch(4 * n + 3, 3);
            if (n < 3) fetch(4 * n + 4, 0);
        };
        auto sync_b = [&]() {
            WAIT_VM_BAR(0);
            if (n < 3) { fetch(4 * n + 5, 1); fetch(4 * n + 6, 2); }
        };
        CHUNK(0, A_SLOT, sync_a());
        CHUNK(A_SLOT, 2 * A_SLOT, (void)0);
        CHUNK(2 * A_SLOT, 3 * A_SLOT, sync_b());
        CHUNK(3 * A_SLOT, 0, (void)0);
    }
    oB = MFMA32(v1, BC8(p1), oB);
    STAMP(0, 5);
    {
        LAS float* ex = (LAS float*)(L + A_SLOT + wid * 2560) + lane;
#pragma unroll
        for (int i = 0; i < 9; ++i) ex[i * 64] = kh ? oA[i] : oB[i];
        ex[9 * 64] = mref;
    }
    LGKM_BAR();
    f32x16 o;
    {
        LAS const float* ex = (LAS const float*)(L + A_SLOT + (wid ^ 4) * 2560) + lane;
        const float mo = ex[9 * 64], mm = fmaxf(mref, mo);
        const float f1 = __builtin_amdgcn_exp2f(mref - mm), f2 = __builtin_amdgcn_exp2f(mo - mm);
#pragma unroll
        for (int i = 0; i < 9; ++i) o[i] = (kh ? oB[i] : oA[i]) * f1 + ex[i * 64] * f2;
        bool ok = (kh ? oB[8] : oA[8]) < 1.0995116e12f && ex[8 * 64] < 1.0995116e12f && o[8] > 9.0949470e-13f;
#pragma unroll
        for (int i = 0; i < 8; ++i) ok = ok && (fabsf(o[i]) < 3.0e38f);
        if (__builtin_expect(!__all(ok), 0)) o = attn_exact(rs, WS_KV + (unsigned)hp * 16 * 16384 + hsel * 2048, kh ? qB : qA, lane);
    }
    {
        const float inv = 1.f / o[8];
        u32x4 w;
#pragma unroll
        for (int j = 0; j < 4; ++j) w[j] = cvtpk(o[2 * j] * inv, o[2 * j + 1] * inv);
        const int t32 = qb * 4 + qp * 2 + kh;
        st16_dev(rs, WS_O + (unsigned)((2 * t32 + (r32 >> 4)) * 16 + hp) * 1024 + ((2 * hsel + hh) * 16 + (r32 & 15)) * 16, w);
        WAIT_VM(0);
        if (lane == 0) flag_set(ws, WS_OF + (unsigned)(t32 * 32 + 2 * hp + hsel) * 8, tag);
    }
    STAMP(0, 6);
}

DI void role_f(int blk, const u16* __restrict__ x, const u16* __restrict__ wo, const u16* __restrict__ wg, const u16* __restrict__ wu,
               const u16* __restrict__ wd, const u16* __restrict__ gw, char* __restrict__ ws, u16* __restrict__ out, char* lds) {
    const int wid = __builtin_amdgcn_readfirstlane(threadIdx.x >> 6);
    const int xcd = blk & 7, bi = blk >> 3;
    const int cq = xcd >> 1, rb = (xcd & 1) * 8 + (bi & 7), jj = bi >> 3;
    const int funit = cq * 14 + (jj < 2 ? 3 * jj : 4 * jj - 2);
    const int mb = 2 * rb + (cq >> 1), nb = (cq & 1) * 4 + jj;
    const unsigned lds0 = (unsigned)(uintptr_t)lds;
    LAS char* L = (LAS char*)lds;
    const rsrc_t rs = __builtin_amdgcn_make_buffer_rsrc(ws, 0, WS_END, 0x00020000);
    STAMP(1, 0);
    f32x4 acc[4][4];
#pragma unroll
    for (int a = 0; a < 4; ++a)
#pragma unroll
        for (int b = 0; b < 4; ++b) acc[a][b] = (f32x4){0.f, 0.f, 0.f, 0.f};
    u64 tag;
    {
        LANE16();
        const int wm = wid >> 2, wn = wid & 3;
        const bool active = jj >= 2 || wn < 3;
        const int xrow = 8 * wid + (lane >> 3);
        const unsigned xo = (unsigned)(xrow * 1024 + (((lane & 7) ^ ((xrow >> 1) & 7)) << 4));
        unsigned wcol = (unsigned)((((r16 >> 1) ^ (g4 | (((wid >> 1) & 1) << 2))) << 5) + (lane & 1) * 16);
        if (jj < 2 && wcol >= 192) wcol -= 64;
        const unsigned wo_ = (unsigned)((4 * wid + g4) * (FF * 2)) + wcol;
        const char* xb = (const char*)x + (size_t)rb * 128 * 1024;
        const char* gb = (const char*)wg + funit * 64;
        const char* ub = (const char*)wu + funit * 64;
        const unsigned dw = lds0 + wid * 1024;
        auto ld_x = [&](int s) { const unsigned d = dw + (s & 1) * 16384; dma16(xo, xb + s * 128, d); dma16(xo, xb + 65536 + s * 128, d + 8192); };
        auto ld_w = [&](int j, int slot) { const unsigned d = dw + 32768 + slot * 16384; const size_t o = (size_t)j * (32 * FF * 2); dma16(wo_, gb + o, d); dma16(wo_, ub + o, d + 8192); };
        ld_x(0); ld_w(0, 0); ld_w(1, 1); ld_x(1); ld_w(2, 2);
        tag = read_tag(ws);
        const int xr0 = (wm * 64 + r16) * 128 + ((g4 ^ (r16 >> 1)) << 4);
        const int wr0 = 32768 + (8 * g4 + q4) * 256 + (((2 * wn) ^ (q4 | ((g4 & 1) << 2))) << 5) + p4 * 8;
        bf16x8 xf[4], wf[2][4];
        auto rd_w = [&](int base, int f) { const int a = (base ^ ((f & 1) << 5)) + (f >> 1) * 8192; return cat8(tr_read(L + a), tr_read(L + a + 1024)); };
        WAIT_VM_BAR(6);
        STAMP(1, 1);
#pragma unroll
        for (int t = 0; t < 4; ++t) { xf[t] = LD8(xr0 + t * 2048); wf[0][t] = rd_w(wr0, t); }
        int ws_cur = 0;
        auto step = [&](auto CU, int xnext, int wnext, bool more) {
            constexpr int cu = decltype(CU)::value;
            if (active) {
                static_for<0, 4>([&](auto T) {
                    constexpr int t = decltype(T)::value;
#pragma unroll
                    for (int f = 0; f < 4; ++f) acc[t][f] = MFMA16(wf[cu][f], xf[t], acc[t][f]);
                    SBAR();
                    if (more) { xf[t] = LD8(xnext + t * 2048); wf[cu ^ 1][t] = rd_w(wnext, t); }
                    SBAR();
                });
            }
        };
#pragma unroll 1
        for (int s = 0; s < 8; ++s) {
            if (s < 7) WAIT_VM_BAR(4); else WAIT_VM_BAR(0);
            if (2 * s + 3 < 16) ld_w(2 * s + 3, ws_cur);
            ws_cur = ws_cur == 2 ? 0 : ws_cur + 1;
            step(IC<0>{}, ((s & 1) * 16384 + xr0) ^ 64, wr0 + ws_cur * 16384, true);
            WAIT_VM_BAR(2);
            if (s + 2 < 8) { ld_x(s + 2); ld_w(2 * s + 4, ws_cur); }
            ws_cur = ws_cur == 2 ? 0 : ws_cur + 1;
            step(IC<1>{}, ((s + 1) & 1) * 16384 + xr0, wr0 + ws_cur * 16384, s < 7);
        }
        STAMP(1, 2);
        if (active) {
            const int lane = fresh_lane();
#pragma unroll
            for (int t = 0; t < 4; ++t) {
                const f32x4 h0 = acc[t][0] * acc[t][2], h1 = acc[t][1] * acc[t][3];
                const u32x4 v = {cvtpk(h0[0], h0[1]), cvtpk(h0[2], h0[3]), cvtpk(h1[0], h1[1]), cvtpk(h1[2], h1[3])};
                st16_dev(rs, WS_H + (unsigned)((rb * 8 + wm * 4 + t) * 56 + funit + wn) * 1024 + lane * 16, v);
            }
            WAIT_VM(0);
            if (lane == 0) flag_set(ws, WS_HF + (unsigned)((2 * rb + wm) * 64 + funit + wn) * 8, tag);
        }
    }
#define WDO() (unsigned)((lane >> 3) * 1024 + ((((lane & 7) >> 1) ^ ((lane >> 4) & 3)) << 5) + (lane & 1) * 16)
#define SWZ() ((2 * (g4 & 1) + (q4 >> 1)) << 5)
    f32x4 bsum[2];
    {
        LANE16();
        const unsigned wdo = WDO();
        const int swz = SWZ();
        const char* wdb = (const char*)wd + (size_t)(7 * wid) * 32 * 1024 + nb * 128;
        const unsigned dd = lds0 + wid * 8192;
        auto ld_wd = [&](int k) {
#pragma unroll
            for (int e = 0; e < 4; ++e) dma16(wdo, wdb + (size_t)k * 32768 + e * 8192, dd + (k & 1) * 4096 + e * 1024);
        };
        ld_wd(0); ld_wd(1);
#pragma unroll
        for (int a = 0; a < 4; ++a)
#pragma unroll
            for (int b = 0; b < 4; ++b) acc[a][b] = (f32x4){0.f, 0.f, 0.f, 0.f};
        flag_wait(ws, WS_HF + (unsigned)(mb * 64 + 7 * wid + (lane < 7 ? lane : 0)) * 8, tag);
        STAMP(1, 3);
        const unsigned hv = (unsigned)(lane * 16), hs = WS_H + (unsigned)(mb * 4 * 56 + 7 * wid) * 1024;
        u32x4 hf[2][4];
#pragma unroll
        for (int k = 0; k < 2; ++k)
#pragma unroll
            for (int t = 0; t < 4; ++t) hf[k][t] = ld16_dev(rs, hv, hs + (t * 56 + k) * 1024);
        const int rd0 = wid * 8192 + (4 * g4 + q4) * 128 + swz + p4 * 8;
        static_for<0, 7>([&](auto K) {
            constexpr int k = decltype(K)::value;
            if constexpr (k >= 2) { if constexpr (k < 6) WAIT_VM(12); else WAIT_VM(4); }
            bf16x8 wf[4];
#pragma unroll
            for (int c = 0; c < 4; ++c) { const int a = (rd0 ^ (c << 5)) + (k & 1) * 4096; wf[c] = cat8(tr_read(L + a), tr_read(L + a + 2048)); }
#pragma unroll
            for (int t = 0; t < 4; ++t) PIN(hf[k & 1][t]);
#pragma unroll
            for (int c = 0; c < 4; ++c) PIN(wf[c]);
            if constexpr (k + 2 < 7) ld_wd(k + 2);
#pragma unroll
            for (int t = 0; t < 4; ++t)
#pragma unroll
                for (int c = 0; c < 4; ++c) acc[t][c] = MFMA16(wf[c], BC8(hf[k & 1][t]), acc[t][c]);
            SBAR();
            if constexpr (k + 2 < 7) {
#pragma unroll
                for (int t = 0; t < 4; ++t) hf[k & 1][t] = ld16_dev(rs, hv, hs + (t * 56 + k + 2) * 1024);
            }
            SBAR();
        });
        STAMP(1, 4);
#pragma unroll
        for (int r = 0; r < 2; ++r) {
            LGKM_BAR();
#pragma unroll
            for (int t = 0; t < 4; ++t)
#pragma unroll
                for (int c = 0; c < 2; ++c) *(LAS f32x4*)(L + wid * 8192 + (c * 4 + t) * 1024 + lane * 16) = acc[t][2 * c + r];
            LGKM_BAR();
            f32x4 s = *(LAS const f32x4*)(L + wid * 1024 + lane * 16);
#pragma unroll
            for (int w = 1; w < 8; ++w) s += *(LAS const f32x4*)(L + w * 8192 + wid * 1024 + lane * 16);
            bsum[r] = s;
        }
    }
    LGKM_BAR();
    {
        LANE16();
        const unsigned wdo = WDO();
        const char* wob = (const char*)wo + (size_t)wid * 64 * 1024 + nb * 128;
#pragma unroll
        for (int e = 0; e < 8; ++e) dma16(wdo, wob + e * 8192, lds0 + wid * 8192 + e * 1024);
        const u16* xr = x + (size_t)(mb * 64 + wid * 8 + (lane >> 3)) * DM + (lane & 7) * 8;
        float z = 0.f;
#pragma unroll
        for (int i = 0; i < 8; ++i) {
            const u32x4 xv = *(const u32x4*)(xr + i * 64), gv = *(const u32x4*)(gw + (lane & 7) * 8 + i * 64);
#pragma unroll
            for (int j = 0; j < 4; ++j) {
                z = fmaf(__uint_as_float(xv[j] << 16), __uint_as_float(gv[j] << 16), z);
                z = fmaf(__uint_as_float(xv[j] & 0xffff0000u), __uint_as_float(gv[j] & 0xffff0000u), z);
            }
        }
        z += __shfl_xor(z, 1); z += __shfl_xor(z, 2); z += __shfl_xor(z, 4);
        if ((lane & 7) == 0) *(LAS float*)(L + 65536 + (wid * 8 + (lane >> 3)) * 4) = 1.f / (1.f + __expf(-z));
    }
    if (blk == 0 && wid == 0) {
        const int lane = fresh_lane();
#pragma unroll 1
        for (int i = 0; i < 4; ++i) {
            const int w = i * 64 + lane;
            flag_wait(ws, WS_KVF + (unsigned)(((w >> 4) * 4) * 16 + (w & 15)) * 8, tag);
            const int cj = w & 3;
            flag_wait(ws, WS_HF + (unsigned)((2 * (w >> 4)) * 64 + ((w >> 2) & 3) * 14 + (cj < 2 ? 3 * cj : 4 * cj - 2)) * 8, tag);
        }
        if (lane == 0) flag_set(ws, WS_E, tag);
    }
    WAIT_VM_BAR(0);
    STAMP(1, 5);
    {
        LANE16();
        const int swz = SWZ();
        const int tt = wid & 3, ch = wid >> 2;
        const int tt16 = mb * 4 + tt;
        const int rd0 = (16 * (g4 >> 1) + 4 * (g4 & 1) + q4) * 128 + (swz ^ (ch << 6)) + p4 * 8;
        const float gt = *(LAS const float*)(L + 65536 + (tt * 16 + r16) * 4);
        flag_wait(ws, WS_OF + (unsigned)((tt16 >> 1) * 32 + (lane & 31)) * 8, tag);
        STAMP(1, 6);
        u32x4 of[16];
#pragma unroll
        for (int k = 0; k < 16; ++k) of[k] = ld16_dev(rs, (unsigned)(lane * 16), WS_O + (unsigned)(tt16 * 16 + k) * 1024);
        f32x4 a0 = {0.f, 0.f, 0.f, 0.f}, a1 = a0;
#pragma unroll
        for (int k = 0; k < 16; ++k) {
            const bf16x8 w0 = cat8(tr_read(L + rd0 + k * 4096), tr_read(L + rd0 + k * 4096 + 1024));
            const bf16x8 w1 = cat8(tr_read(L + (rd0 ^ 32) + k * 4096), tr_read(L + (rd0 ^ 32) + k * 4096 + 1024));
            a0 = MFMA16(w0, BC8(of[k]), a0); a1 = MFMA16(w1, BC8(of[k]), a1);
        }
        const float gb = 1.f - gt;
        const f32x4 r0 = a0 * gt + bsum[0] * gb, r1 = a1 * gt + bsum[1] * gb;
        u16* op = out + (size_t)(tt16 * 16 + r16) * DM + nb * 64 + ch * 32 + g4 * 4;
        *(u32x2*)op = (u32x2){cvtpk(r0[0], r0[1]), cvtpk(r0[2], r0[3])};
        *(u32x2*)(op + 16) = (u32x2){cvtpk(r1[0], r1[1]), cvtpk(r1[2], r1[3])};
    }
    STAMP(1, 7);
}

__global__ __launch_bounds__(512, 4) void fused_fwd(const u16* __restrict__ x, const u16* __restrict__ wqkv, const u16* __restrict__ wo,
                                                     const u16* __restrict__ wg, const u16* __restrict__ wu, const u16* __restrict__ wd,
                                                     const u16* __restrict__ gw, char* __restrict__ ws, u16* __restrict__ out) {
    extern __shared__ __attribute__((aligned(16))) char lds[];
    if (blockIdx.x < 256) role_a(blockIdx.x, x, wqkv, ws, lds);
    else role_f(blockIdx.x - 256, x, wo, wg, wu, wd, gw, ws, out, lds);
}

extern "C" void kernel_launch(void* const* d_in, const int* in_sizes, int n_in, void* d_out, int out_size, void* d_ws, size_t ws_size,
                              hipStream_t stream) {
    (void)hipFuncSetAttribute((const void*)fused_fwd, hipFuncAttributeMaxDynamicSharedMemorySize, LDS_BYTES);
    fused_fwd<<<512, 512, LDS_BYTES, stream>>>((const u16*)d_in[0], (const u16*)d_in[1], (const u16*)d_in[2], (const u16*)d_in[4],
                                               (const u16*)d_in[5], (const u16*)d_in[6], (const u16*)d_in[7], (char*)d_ws, (u16*)d_out);
}
```

```cpp
#include <hip/hip_runtime.h>
#include <hip/hip_bf16.h>
#include <stdint.h>

typedef short bf16x8 __attribute__((ext_vector_type(8)));
typedef short s16x4 __attribute__((ext_vector_type(4)));
typedef float f32x16 __attribute__((ext_vector_type(16)));
typedef float f32x4 __attribute__((ext_vector_type(4)));
typedef float f32x2 __attribute__((ext_vector_type(2)));
typedef unsigned u32x4 __attribute__((ext_vector_type(4)));
typedef unsigned u32x2 __attribute__((ext_vector_type(2)));
typedef __bf16 bf16x2_t __attribute__((ext_vector_type(2)));
typedef unsigned short u16;
typedef unsigned long long u64;
#define LAS __attribute__((address_space(3)))
#define GAS __attribute__((address_space(1)))
#define DI __device__ __forceinline__
#define MFMA32(a, b, c) __builtin_amdgcn_mfma_f32_32x32x16_bf16((a), (b), (c), 0, 0, 0)
#define MFMA16(a, b, c) __builtin_amdgcn_mfma_f32_16x16x32_bf16((a), (b), (c), 0, 0, 0)
#define SBAR() __builtin_amdgcn_sched_barrier(0)
#define PIN(x) asm volatile("" : "+v"(x))
#define BC8(x) __builtin_bit_cast(bf16x8, x)
template <int V> struct IC { static constexpr int value = V; };
template <int I, int N, class F> DI void static_for(F&& f) { if constexpr (I < N) { f(IC<I>{}); static_for<I + 1, N>(f); } }

constexpr int SEQ = 2048, DM = 512, FF = 1792;
constexpr unsigned WS_E = 0;
constexpr unsigned WS_KVF = 4096;
constexpr unsigned WS_HF = WS_KVF + 8192;
constexpr unsigned WS_OF = WS_HF + 16384;
constexpr unsigned WS_G = WS_OF + 16384;
constexpr unsigned WS_KV = 65536;
constexpr unsigned WS_H = WS_KV + (4u << 20);
constexpr unsigned WS_O = WS_H + 128u * 56 * 1024;
constexpr unsigned WS_END = WS_O + 128u * 16 * 1024;
constexpr float QSCALE = 0.25f * 1.4426950408889634f;
constexpr int LDS_BYTES = 81920;

DI unsigned cvtpk(float lo, float hi) { const f32x2 v = {lo, hi}; return __builtin_bit_cast(unsigned, __builtin_convertvector(v, bf16x2_t)); }
DI s16x4 tr_read(LAS const char* p) { return __builtin_amdgcn_ds_read_tr16_b64_v4i16((LAS s16x4*)p); }
DI bf16x8 cat8(s16x4 a, s16x4 b) { return __builtin_shufflevector(a, b, 0, 1, 2, 3, 4, 5, 6, 7); }
DI void dma16(unsigned voff, const void* sbase, unsigned lds) {
    asm volatile("s_mov_b32 m0, %2\n\ts_nop 0\n\tglobal_load_lds_dwordx4 %0, %1" :: "v"(voff), "s"(sbase), "s"(lds) : "memory");
}
DI void dma16_dev(unsigned voff, const void* sbase, unsigned lds) {
    asm volatile("s_mov_b32 m0, %2\n\ts_nop 0\n\tglobal_load_lds_dwordx4 %0, %1 sc1" :: "v"(voff), "s"(sbase), "s"(lds) : "memory");
}
DI void dma16_48(unsigned voff, const void* sbase, unsigned lds) {
    asm volatile("s_mov_b32 m0, %2\n\ts_mov_b32 exec_hi, 0xffff\n\tglobal_load_lds_dwordx4 %0, %1\n\ts_mov_b32 exec_hi, -1" :: "v"(voff), "s"(sbase), "s"(lds) : "memory");
}
template <int N> DI void wait_vm_bar() { asm volatile("s_waitcnt vmcnt(%0) lgkmcnt(0)\n\ts_barrier" :: "n"(N) : "memory"); }
#define WAIT_VM(N) asm volatile("s_waitcnt vmcnt(" #N ")" ::: "memory")
#define WAIT_VM_BAR(N) asm volatile("s_waitcnt vmcnt(" #N ") lgkmcnt(0)\n\ts_barrier" ::: "memory")
#define LGKM_BAR() asm volatile("s_waitcnt lgkmcnt(0)\n\ts_barrier" ::: "memory")
#define BAR() asm volatile("s_barrier" ::: "memory")
typedef __amdgpu_buffer_rsrc_t rsrc_t;
DI void st16_dev(rsrc_t r, unsigned off, u32x4 v) { __builtin_amdgcn_raw_buffer_store_b128(v, r, off, 0, 16); }
DI u32x4 ld16_dev(rsrc_t r, unsigned voff, unsigned soff) { return __builtin_amdgcn_raw_buffer_load_b128(r, voff, soff, 16); }
DI void flag_set(char* ws, unsigned off, u64 tag) { __hip_atomic_store((GAS u64*)(ws + off), tag, __ATOMIC_RELAXED, __HIP_MEMORY_SCOPE_AGENT); }
template <int NAP, int LONG_NAP = NAP> DI void flag_wait(const char* ws, unsigned off, u64 tag) {
    const u64 t0 = __builtin_amdgcn_s_memrealtime();
    for (;;) {
        const u64 v = __hip_atomic_load((const GAS u64*)(ws + off), __ATOMIC_RELAXED, __HIP_MEMORY_SCOPE_AGENT);
        const u64 got = __ballot(v == tag);
        if (got == ~0ull) break;
        if (__builtin_amdgcn_s_memrealtime() - t0 > 2000000ull) break;
        if (LONG_NAP != NAP && got == 0) __builtin_amdgcn_s_sleep(LONG_NAP); else __builtin_amdgcn_s_sleep(NAP);
    }
    asm volatile("" ::: "memory");
}
DI u64 read_tag(const char* ws) {
    u64 e;
    asm volatile("s_load_dwordx2 %0, %1, 0x0\n\ts_waitcnt lgkmcnt(0)" : "=s"(e) : "s"(ws) : "memory");
    return e * 6364136223846793005ull + 1442695040888963407ull;
}

DI int fresh_lane() { int l; asm volatile("v_mbcnt_lo_u32_b32 %0, -1, 0\n\tv_mbcnt_hi_u32_b32 %0, -1, %0" : "=v"(l)); return l; }
#define LANE16() const int lane = fresh_lane(), r16 = lane & 15, g4 = lane >> 4, q4 = r16 >> 2, p4 = lane & 3; (void)r16; (void)g4; (void)q4; (void)p4

#define STAMP(k, i) do { } while (0)

constexpr int P_W = 49152;
constexpr int P_GW = P_W + 5 * 6144;
constexpr int A_SLOT = 17408;
constexpr int A_Q = 4 * A_SLOT;

DI f32x16 attn_exact(rsrc_t rs, unsigned kvbase, bf16x8 qf, int lane) {
    const int r32 = lane & 31, hh = lane >> 5;
    f32x16 o, zero;
#pragma unroll
    for (int i = 0; i < 16; ++i) { o[i] = 0.f; zero[i] = 0.f; }
    float m = -INFINITY;
    const unsigned cst = ((r32 & 27) == 16) ? 0x3f803f80u : 0u;
    const u32x4 cv = {cst, cst, cst, cst};
#pragma unroll 1
    for (int t = 0; t < 64; ++t) {
        const unsigned tb = kvbase + (t >> 2) * 16384 + ((t >> 1) & 1) * 4096;
        const bf16x8 kf = BC8(ld16_dev(rs, tb + (t & 1) * 1024 + lane * 16, 0));
        const f32x16 s = MFMA32(kf, qf, zero);
        float tm = s[0];
#pragma unroll
        for (int i = 1; i < 16; ++i) tm = fmaxf(tm, s[i]);
        tm = fmaxf(tm, __shfl_xor(tm, 32));
        const float mn = fmaxf(m, tm);
        const float alpha = __builtin_amdgcn_exp2f(m - mn);
        m = mn;
#pragma unroll
        for (int i = 0; i < 16; ++i) o[i] *= alpha;
#pragma unroll
        for (int st = 0; st < 2; ++st) {
            u32x4 pw;
#pragma unroll
            for (int j = 0; j < 4; ++j) pw[j] = cvtpk(__builtin_amdgcn_exp2f(s[8 * st + 2 * j] - m), __builtin_amdgcn_exp2f(s[8 * st + 2 * j + 1] - m));
            const u32x4 vv = ld16_dev(rs, tb + 8192 + hh * 1024 + ((t & 1) * 2 + st) * 256 + (r32 & 15) * 16, 0);
            o = MFMA32(BC8(r32 < 16 ? vv : cv), BC8(pw), o);
        }
    }
    return o;
}

DI void role_a(int blk, const u16* __restrict__ x, const u16* __restrict__ wqkv, const u16* __restrict__ gw, char* __restrict__ ws, char* lds) {
    const int lane = fresh_lane(), wid = __builtin_amdgcn_readfirstlane(threadIdx.x >> 6);
    const int r32 = lane & 31, hh = lane >> 5;
    const int xcd = blk & 7, bi = blk >> 3;
    const int qb = (xcd & 1) * 8 + (bi & 7), hp = (xcd >> 1) * 4 + (bi >> 3);
    const unsigned lds0 = (unsigned)(uintptr_t)lds;
    LAS char* L = (LAS char*)lds;
    const rsrc_t rs = __builtin_amdgcn_make_buffer_rsrc(ws, 0, WS_END, 0x00020000);
    const unsigned kvown = WS_KV + (unsigned)(hp * 16 + qb) * 16384;
    u64 tag;
    STAMP(0, 0);
    __builtin_amdgcn_s_setprio(3);
#define LD8(A) (*(LAS const bf16x8*)(L + (A)))
    {
        const int xrow = 8 * wid + (lane >> 3);
        const unsigned xo = (unsigned)(xrow * 1024 + (((lane & 7) ^ ((xrow >> 1) & 7)) << 4));
        const unsigned wo_ = (unsigned)((4 * wid + ((lane >> 2) & 3)) * 3072 + (lane >> 4) * 1024 + (lane & 3) * 16);
        const char* xb = (const char*)x + (size_t)qb * 128 * 1024;
        const char* wb = (const char*)wqkv + hp * 64;
        auto ld_x = [&](int h) { dma16(xo, xb + (h & 1) * 65536 + (h >> 1) * 128, lds0 + (((h >> 1) + 1) % 3) * 16384 + (h & 1) * 8192 + wid * 1024); };
        auto ld_w = [&](int j) { dma16_48(wo_, wb + (size_t)j * (32 * 3072), lds0 + P_W + (j % 5) * 6144 + wid * 768); };
        if (wid == 7) dma16((unsigned)(lane * 16), gw, lds0 + P_GW);
#pragma unroll
        for (int j = 0; j < 5; ++j) { ld_x(j); ld_w(j); }
        ld_x(5);
        tag = read_tag(ws);
        auto proj = [&](auto KIND) {
            constexpr int kind = decltype(KIND)::value, NT = kind == 0 ? 1 : 2;
            const int row0 = kind == 0 ? (wid - 4) * 32 : (wid & 1) * 64;
            int xa[4];
#pragma unroll
            for (int k = 0; k < 4; ++k) { xa[k] = ((row0 + r32) * 128 + ((hh ^ ((r32 >> 1) & 7)) << 4)) ^ (k << 5); PIN(xa[k]); }
            const int wa = P_W + hh * 1536 + kind * 256 + ((lane & 15) >> 2) * 64 + ((lane >> 4) & 1) * 32 + (lane & 3) * 8;
            const int ga = (8 * hp + (lane >> 3)) * 128 + (((lane & 7) ^ ((4 * hp + (lane >> 4)) & 7)) << 4);
            f32x16 acc[NT];
#pragma unroll
            for (int t = 0; t < NT; ++t)
#pragma unroll
                for (int i = 0; i < 16; ++i) acc[t][i] = 0.f;
            bf16x8 xf[2][2][NT], wf[2][2];
            u32x4 gx, gv;
            float z = 0.f;
            static_for<0, 17>([&](auto J) {
                constexpr int j = decltype(J)::value, c = j & 1;
                if constexpr (j < 16) {
                    constexpr int issued = j < 2 ? 11 : (2 * j + 8 < 32 ? 2 * j + 8 : 32);
                    wait_vm_bar<issued - 1 - (c ? 2 * j + 1 : 2 * j + 2)>();
                    constexpr int xs = (((j >> 1) + 1) % 3) * 16384, wsl = (j % 5) * 6144;
#pragma unroll
                    for (int ks = 0; ks < 2; ++ks) {
                        wf[c][ks] = cat8(tr_read(L + wa + wsl + ks * 3072), tr_read(L + wa + wsl + ks * 3072 + 768));
#pragma unroll
                        for (int t = 0; t < NT; ++t) xf[c][ks][t] = LD8(xa[2 * c + ks] + xs + t * 4096);
                    }
                    if (kind == 0 && c == 0 && wid == 7) { gx = *(LAS const u32x4*)(L + ga + xs); gv = *(LAS const u32x4*)(L + P_GW + (j >> 1) * 128 + (lane & 7) * 16); }
                    SBAR();
                }
#pragma unroll
                for (int ks = 0; ks < 2; ++ks) {
                    if constexpr (j > 0) {
#pragma unroll
                        for (int t = 0; t < NT; ++t)
                            acc[t] = kind == 2 ? MFMA32(xf[c ^ 1][ks][t], wf[c ^ 1][ks], acc[t]) : MFMA32(wf[c ^ 1][ks], xf[c ^ 1][ks][t], acc[t]);
                    }
                    SBAR();
                    if constexpr (j + 4 < 16) { if (ks == 0) { if (j >= 2) ld_x(j + 4); } else if (j >= 1) ld_w(j + 4); }
                    SBAR();
                }
                if (kind == 0 && c == 1 && wid == 7) {
#pragma unroll
                    for (int e = 0; e < 4; ++e) {
                        z = fmaf(__uint_as_float(gx[e] << 16), __uint_as_float(gv[e] << 16), z);
                        z = fmaf(__uint_as_float(gx[e] & 0xffff0000u), __uint_as_float(gv[e] & 0xffff0000u), z);
                    }
                }
            });
#pragma unroll
            for (int i = 0; i < NT; ++i)
#pragma unroll
                for (int e = 0; e < 2; ++e) {
                    u32x4 v;
#pragma unroll
                    for (int j = 0; j < 4; ++j)
                        v[j] = kind == 0 ? cvtpk(acc[i][8 * e + 2 * j] * QSCALE, acc[i][8 * e + 2 * j + 1] * QSCALE) : cvtpk(acc[i][8 * e + 2 * j], acc[i][8 * e + 2 * j + 1]);
                    if constexpr (kind == 0) *(LAS u32x4*)(L + A_Q + ((wid - 4) * 2 + e) * 1024 + lane * 16) = v;
                    else {
                        const int tg = wid & 1;
                        const int off = kind == 1 ? tg * 4096 + e * 2048 + i * 1024 + lane * 16
                                                  : 8192 + tg * 4096 + (r32 >> 4) * 2048 + hh * 1024 + (2 * i + e) * 256 + (r32 & 15) * 16;
                        st16_dev(rs, kvown + off, v);
                        *(LAS u32x4*)(L + off) = v;
                    }
                }
            if (kind == 0 && wid == 7) {
                z += __shfl_xor(z, 1); z += __shfl_xor(z, 2); z += __shfl_xor(z, 4);
                if ((lane & 7) == 0) __builtin_amdgcn_raw_buffer_store_b32(__float_as_uint(1.f / (1.f + __expf(-z))), rs, WS_G + (unsigned)(qb * 128 + 8 * hp + (lane >> 3)) * 4, 0, 16);
            }
        };
        if (wid >= 4) proj(IC<0>{}); else if (wid < 2) proj(IC<1>{}); else proj(IC<2>{});
    }
    const unsigned cst = ((lane & 11) == 0) ? 0x3f803f80u : 0u;
    if (wid == 4) *(LAS u32x4*)(L + 16384 + lane * 16) = (u32x4){cst, cst, cst, cst};
    LGKM_BAR();
    STAMP(0, 1);
    if (wid > 4) *(LAS u32x4*)(L + (wid - 4) * A_SLOT + 16384 + lane * 16) = (u32x4){cst, cst, cst, cst};
    const int hsel = wid & 1, qp = (wid >> 1) & 1, kh = wid >> 2;
    const bf16x8 qA = *(LAS const bf16x8*)(L + A_Q + ((2 * qp) * 2 + hsel) * 1024 + lane * 16);
    const bf16x8 qB = *(LAS const bf16x8*)(L + A_Q + ((2 * qp + 1) * 2 + hsel) * 1024 + lane * 16);
    int ka = kh * 4096 + hsel * 2048 + lane * 16;
    int va = r32 < 16 ? 8192 + kh * 4096 + hsel * 2048 + hh * 1024 + r32 * 16 : 16384 + (r32 & 15) * 16;
    PIN(ka); PIN(va);
    bf16x8 kf = LD8(ka);
    f32x16 negm, oA, oB, sX, sY;
    float mref;
    {
        f32x16 zero;
#pragma unroll
        for (int i = 0; i < 16; ++i) { zero[i] = 0.f; oA[i] = 0.f; oB[i] = 0.f; }
        sX = MFMA32(kf, qA, zero); sY = MFMA32(kf, qB, zero);
        float tm = fmaxf(sX[0], sY[0]);
#pragma unroll
        for (int i = 1; i < 16; ++i) tm = fmaxf(tm, fmaxf(sX[i], sY[i]));
        mref = fmaxf(tm, __shfl_xor(tm, 32));
#pragma unroll
        for (int i = 0; i < 16; ++i) negm[i] = -mref;
        PIN(negm);
    }
    sX = MFMA32(kf, qA, negm);
    bf16x8 v0 = LD8(va), v1 = v0;
    u32x4 p0, p1 = {0u, 0u, 0u, 0u};
    if (wid < 4) {
        WAIT_VM(0);
        if (lane == 0) flag_set(ws, WS_KVF + (unsigned)((hp * 4 + wid) * 16 + qb) * 8, tag);
    }
    __builtin_amdgcn_s_setprio(0);
    const char* kvg = ws + WS_KV + (size_t)hp * 16 * 16384 + wid * 2048;
    const unsigned doff = (unsigned)(lane * 16);
    auto fetch = [&](int c, int slot) {
        const char* b = kvg + (size_t)((qb + c) & 15) * 16384;
        const unsigned d = lds0 + slot * A_SLOT + wid * 2048;
        dma16_dev(doff, b, d); dma16_dev(doff, b + 1024, d + 1024);
    };
#define EX2(X, i) X[i] = __builtin_amdgcn_exp2f(X[i])
#define HALF(SC, M1, L1, M2, L2, M3, L3) do { \
        EX2(SC, 0); EX2(SC, 1); PIN(SC); SBAR(); \
        M1; SBAR(); L1; \
        EX2(SC, 2); EX2(SC, 3); EX2(SC, 4); EX2(SC, 5); EX2(SC, 6); EX2(SC, 7); PIN(SC); SBAR(); \
        M2; SBAR(); L2; \
        p0[0] = cvtpk(SC[0], SC[1]); p0[1] = cvtpk(SC[2], SC[3]); p0[2] = cvtpk(SC[4], SC[5]); p0[3] = cvtpk(SC[6], SC[7]); PIN(p0); SBAR(); \
        EX2(SC, 8); EX2(SC, 9); EX2(SC, 10); EX2(SC, 11); PIN(SC); SBAR(); \
        M3; SBAR(); L3; \
        EX2(SC, 12); EX2(SC, 13); EX2(SC, 14); EX2(SC, 15); \
        p1[0] = cvtpk(SC[8], SC[9]); p1[1] = cvtpk(SC[10], SC[11]); p1[2] = cvtpk(SC[12], SC[13]); p1[3] = cvtpk(SC[14], SC[15]); PIN(p1); SBAR(); \
    } while (0)
#define CHUNK(S0, S1, SYNC) do { \
        HALF(sX, oB = MFMA32(v1, BC8(p1), oB), v1 = LD8(va + (S0) + 256), sY = MFMA32(kf, qB, negm), kf = LD8(ka + (S0) + 1024), oA = MFMA32(v0, BC8(p0), oA), (void)0); \
        HALF(sY, oA = MFMA32(v1, BC8(p1), oA), (void)0, sX = MFMA32(kf, qA, negm), (void)0, oB = MFMA32(v0, BC8(p0), oB), v0 = LD8(va + (S0) + 512)); \
        HALF(sX, oB = MFMA32(v1, BC8(p1), oB), v1 = LD8(va + (S0) + 768), sY = MFMA32(kf, qB, negm), SYNC; kf = LD8(ka + (S1)), oA = MFMA32(v0, BC8(p0), oA), (void)0); \
        HALF(sY, oA = MFMA32(v1, BC8(p1), oA), (void)0, sX = MFMA32(kf, qA, negm), (void)0, oB = MFMA32(v0, BC8(p0), oB), v0 = LD8(va + (S1))); \
    } while (0)
#pragma unroll 1
    for (int n = 0; n < 4; ++n) {
        auto sync_a = [&]() {
            if (n == 0) {
                STAMP(0, 2);
                flag_wait<2>(ws, WS_KVF + (unsigned)((hp * 4 + (wid >> 1)) * 16 + (lane & 15)) * 8, tag);
                STAMP(0, 3);
                fetch(1, 1); fetch(2, 2);
            }
            STAMP(2, 4 * n);
            WAIT_VM_BAR(0);
            STAMP(2, 4 * n + 1);
            if (n == 0) STAMP(0, 4);
            fetch(4 * n + 3, 3);
            if (n < 3) fetch(4 * n + 4, 0);
        };
        auto sync_b = [&]() {
            STAMP(2, 4 * n + 2);
            WAIT_VM_BAR(0);
            STAMP(2, 4 * n + 3);
            if (n < 3) { fetch(4 * n + 5, 1); fetch(4 * n + 6, 2); }
        };
        CHUNK(0, A_SLOT, sync_a());
        CHUNK(A_SLOT, 2 * A_SLOT, (void)0);
        CHUNK(2 * A_SLOT, 3 * A_SLOT, sync_b());
        CHUNK(3 * A_SLOT, 0, (void)0);
    }
    oB = MFMA32(v1, BC8(p1), oB);
    STAMP(0, 5);
    {
        LAS float* ex = (LAS float*)(L + A_SLOT + wid * 2560) + lane;
#pragma unroll
        for (int i = 0; i < 9; ++i) ex[i * 64] = kh ? oA[i] : oB[i];
        ex[9 * 64] = mref;
    }
    LGKM_BAR();
    f32x16 o;
    {
        LAS const float* ex = (LAS const float*)(L + A_SLOT + (wid ^ 4) * 2560) + lane;
        const float mo = ex[9 * 64], mm = fmaxf(mref, mo);
        const float f1 = __builtin_amdgcn_exp2f(mref - mm), f2 = __builtin_amdgcn_exp2f(mo - mm);
#pragma unroll
        for (int i = 0; i < 9; ++i) o[i] = (kh ? oB[i] : oA[i]) * f1 + ex[i * 64] * f2;
        bool ok = (kh ? oB[8] : oA[8]) < 1.0995116e12f && ex[8 * 64] < 1.0995116e12f && o[8] > 9.0949470e-13f;
#pragma unroll
        for (int i = 0; i < 8; ++i) ok = ok && (fabsf(o[i]) < 3.0e38f);
        if (__builtin_expect(!__all(ok), 0)) o = attn_exact(rs, WS_KV + (unsigned)hp * 16 * 16384 + hsel * 2048, kh ? qB : qA, lane);
    }
    {
        const float inv = 1.f / o[8];
        u32x4 w;
#pragma unroll
        for (int j = 0; j < 4; ++j) w[j] = cvtpk(o[2 * j] * inv, o[2 * j + 1] * inv);
        const int t32 = qb * 4 + qp * 2 + kh;
        st16_dev(rs, WS_O + (unsigned)((2 * t32 + (r32 >> 4)) * 16 + hp) * 1024 + ((2 * hsel + hh) * 16 + (r32 & 15)) * 16, w);
        WAIT_VM(0);
        if (lane == 0) flag_set(ws, WS_OF + (unsigned)(t32 * 32 + 2 * hp + hsel) * 8, tag);
    }
    STAMP(0, 6);
}

#define F_DELAY 40
#define F_PRIO 2
#define F_HI() __builtin_amdgcn_s_setprio(F_PRIO)
#define F_LO() __builtin_amdgcn_s_setprio(0)
DI void role_f(int blk, const u16* __restrict__ x, const u16* __restrict__ wo, const u16* __restrict__ wg, const u16* __restrict__ wu,
               const u16* __restrict__ wd, char* __restrict__ ws, u16* __restrict__ out, char* lds) {
    const int wid = __builtin_amdgcn_readfirstlane(threadIdx.x >> 6);
    const int xcd = blk & 7, bi = blk >> 3;
    const int cq = xcd >> 1, rb = (xcd & 1) * 8 + (bi & 7), jj = bi >> 3;
    const int funit = cq * 14 + (jj < 2 ? 3 * jj : 4 * jj - 2);
    const int mb = 2 * rb + (cq >> 1), nb = (cq & 1) * 4 + jj;
    const unsigned lds0 = (unsigned)(uintptr_t)lds;
    LAS char* L = (LAS char*)lds;
    const rsrc_t rs = __builtin_amdgcn_make_buffer_rsrc(ws, 0, WS_END, 0x00020000);
    STAMP(1, 0);
    F_HI();
    f32x16 acc[2][2];
#pragma unroll
    for (int i = 0; i < 16; ++i) { acc[0][0][i] = 0.f; acc[0][1][i] = 0.f; acc[1][0][i] = 0.f; acc[1][1][i] = 0.f; }
    const u64 tag = read_tag(ws);
    __builtin_amdgcn_s_sleep(F_DELAY);
    {
        LANE16();
        const int r32 = lane & 31, hh = lane >> 5;
        const int wm = wid >> 2, wn = wid & 3;
        const bool active = jj >= 2 || wn < 3;
        const int xrow = 8 * wid + (lane >> 3);
        const unsigned xo = (unsigned)(xrow * 1024 + (((lane & 7) ^ ((xrow >> 1) & 7)) << 4));
        unsigned wcol = (unsigned)((((r16 >> 1) ^ (2 * g4)) << 5) + (lane & 1) * 16);
        if (jj < 2 && wcol >= 192) wcol -= 64;
        const unsigned wo_ = (unsigned)((4 * wid + g4) * (FF * 2)) + wcol;
        const char* xb = (const char*)x + (size_t)rb * 128 * 1024;
        const char* gb = (const char*)wg + funit * 64;
        const char* ub = (const char*)wu + funit * 64;
        const unsigned dw = lds0 + wid * 1024;
        auto ld_x = [&](int s) { const unsigned d = dw + (s & 1) * 16384; dma16(xo, xb + s * 128, d); dma16(xo, xb + 65536 + s * 128, d + 8192); };
        auto ld_w = [&](int j) { const unsigned d = dw + 32768 + (j % 3) * 16384; const size_t o = (size_t)j * (32 * FF * 2); dma16(wo_, gb + o, d); dma16(wo_, ub + o, d + 8192); };
        ld_x(0); ld_w(0); ld_w(1); ld_x(1); ld_w(2);
        int xa[4];
#pragma unroll
        for (int k = 0; k < 4; ++k) { xa[k] = ((wm * 64 + r32) * 128 + ((hh ^ ((r32 >> 1) & 7)) << 4)) ^ (k << 5); PIN(xa[k]); }
        const int wa = 32768 + (8 * hh + q4) * 256 + (((2 * wn + (g4 & 1)) ^ (2 * q4)) << 5) + p4 * 8;
        bf16x8 x0[2], w0[2], x1[2][2], w1[2][2];
        auto rd = [&](auto J, int ks, bf16x8 (&xf)[2], bf16x8 (&wf)[2]) {
            constexpr int j = decltype(J)::value;
#pragma unroll
            for (int e = 0; e < 2; ++e) {
                const int a = wa + (j % 3) * 16384 + e * 8192 + ks * 4096;
                wf[e] = cat8(tr_read(L + a), tr_read(L + a + 1024));
                xf[e] = LD8(xa[2 * (j & 1) + ks] + ((j >> 1) & 1) * 16384 + e * 4096);
            }
        };
        auto mm = [&](bf16x8 (&xf)[2], bf16x8 (&wf)[2]) {
#pragma unroll
            for (int t = 0; t < 2; ++t)
#pragma unroll
                for (int e = 0; e < 2; ++e) acc[t][e] = MFMA32(wf[e], xf[t], acc[t][e]);
        };
        F_LO();
        WAIT_VM_BAR(6);
        STAMP(1, 1);
        rd(IC<0>{}, 0, x0, w0); rd(IC<0>{}, 1, x1[0], w1[0]);
        static_for<0, 16>([&](auto J) {
            constexpr int j = decltype(J)::value, c = j & 1;
            wait_vm_bar<j >= 14 ? 0 : (c ? 2 : 4)>();
            if constexpr (c == 0) { if constexpr (j + 3 < 16) ld_w(j + 3); }
            else if constexpr (j + 3 < 16) { ld_x((j + 3) >> 1); ld_w(j + 3); }
            if (active) {
                if constexpr (j < 15) rd(IC<j + 1>{}, 1, x1[c ^ 1], w1[c ^ 1]);
                SBAR();
                mm(x0, w0);
                SBAR();
                if constexpr (j < 15) rd(IC<j + 1>{}, 0, x0, w0);
                SBAR();
                mm(x1[c], w1[c]);
                SBAR();
            }
        });
        F_HI();
        STAMP(1, 2);
        if (active) {
            const int lane = fresh_lane();
#pragma unroll
            for (int t = 0; t < 2; ++t)
#pragma unroll
                for (int e = 0; e < 2; ++e) {
                    u32x4 v;
#pragma unroll
                    for (int j = 0; j < 4; ++j) v[j] = cvtpk(acc[t][0][8 * e + 2 * j] * acc[t][1][8 * e + 2 * j], acc[t][0][8 * e + 2 * j + 1] * acc[t][1][8 * e + 2 * j + 1]);
                    st16_dev(rs, WS_H + (unsigned)((rb * 4 + wm * 2 + t) * 112 + 2 * (funit + wn) + e) * 1024 + lane * 16, v);
                }
            WAIT_VM(0);
            if (lane == 0) flag_set(ws, WS_HF + (unsigned)((2 * rb + wm) * 64 + funit + wn) * 8, tag);
        }
    }
#define WDO() (unsigned)((lane >> 3) * 1024 + ((((lane & 7) >> 1) ^ (((lane >> 3) & 2) | (lane >> 5))) << 5) + (lane & 1) * 16)
    f32x4 bsum[2];
    {
        LANE16();
        const int r32 = lane & 31, hh = lane >> 5;
        const unsigned wdo = WDO();
        const char* wdb = (const char*)wd + (size_t)(7 * wid) * 32 * 1024 + nb * 128;
        const unsigned dd = lds0 + wid * 8192;
        auto ld_wd = [&](int k) {
#pragma unroll
            for (int e = 0; e < 4; ++e) dma16(wdo, wdb + (size_t)k * 32768 + e * 8192, dd + (k & 1) * 4096 + e * 1024);
        };
        ld_wd(0); ld_wd(1);
#pragma unroll
        for (int i = 0; i < 16; ++i) { acc[0][0][i] = 0.f; acc[0][1][i] = 0.f; acc[1][0][i] = 0.f; acc[1][1][i] = 0.f; }
        flag_wait<8>(ws, WS_HF + (unsigned)(mb * 64 + 7 * wid + (lane < 7 ? lane : 0)) * 8, tag);
        STAMP(1, 3);
        const unsigned hv = (unsigned)(lane * 16), hs = WS_H + (unsigned)(mb * 2 * 112 + 14 * wid) * 1024;
        u32x4 hf[2][4];
#pragma unroll
        for (int k = 0; k < 2; ++k)
#pragma unroll
            for (int t = 0; t < 4; ++t) hf[k][t] = ld16_dev(rs, hv, hs + ((t >> 1) * 112 + 2 * k + (t & 1)) * 1024);
        int ra[2];
#pragma unroll
        for (int c = 0; c < 2; ++c) { ra[c] = wid * 8192 + (4 * hh + q4) * 128 + (((2 * c + (g4 & 1)) ^ ((q4 & 2) | hh)) << 5) + p4 * 8; PIN(ra[c]); }
        F_LO();
        static_for<0, 7>([&](auto K) {
            constexpr int k = decltype(K)::value;
            if constexpr (k >= 2) { if constexpr (k < 6) WAIT_VM(12); else WAIT_VM(4); }
            bf16x8 wf[4];
#pragma unroll
            for (int c = 0; c < 4; ++c) { const int a = ra[c >> 1] + (k & 1) * 4096 + (c & 1) * 2048; wf[c] = cat8(tr_read(L + a), tr_read(L + a + 1024)); }
#pragma unroll
            for (int t = 0; t < 4; ++t) PIN(hf[k & 1][t]);
#pragma unroll
            for (int c = 0; c < 4; ++c) PIN(wf[c]);
            if constexpr (k + 2 < 7) ld_wd(k + 2);
#pragma unroll
            for (int ks = 0; ks < 2; ++ks)
#pragma unroll
                for (int t = 0; t < 2; ++t)
#pragma unroll
                    for (int c = 0; c < 2; ++c) acc[t][c] = MFMA32(wf[2 * c + ks], BC8(hf[k & 1][2 * t + ks]), acc[t][c]);
            SBAR();
            if constexpr (k + 2 < 7) {
#pragma unroll
                for (int t = 0; t < 4; ++t) hf[k & 1][t] = ld16_dev(rs, hv, hs + ((t >> 1) * 112 + 2 * (k + 2) + (t & 1)) * 1024);
            }
            SBAR();
        });
        F_HI();
        STAMP(1, 4);
#pragma unroll
        for (int r = 0; r < 2; ++r) {
            if (r) LGKM_BAR();
#pragma unroll
            for (int t = 0; t < 2; ++t)
#pragma unroll
                for (int c = 0; c < 2; ++c)
#pragma unroll
                    for (int e = 0; e < 2; ++e) {
                        const int i = 8 * r + 4 * e;
                        *(LAS f32x4*)(L + wid * 8192 + ((c * 4 + 2 * e + hh) * 64 + t * 32 + r32) * 16) = (f32x4){acc[t][c][i], acc[t][c][i + 1], acc[t][c][i + 2], acc[t][c][i + 3]};
                    }
            LGKM_BAR();
            const int ro = (((wid >> 2) * 4 + g4) * 64 + (wid & 3) * 16 + r16) * 16;
            f32x4 s = *(LAS const f32x4*)(L + ro);
#pragma unroll
            for (int w = 1; w < 8; ++w) s += *(LAS const f32x4*)(L + w * 8192 + ro);
            bsum[r] = s;
        }
    }
    LGKM_BAR();
    {
        const int lane = fresh_lane();
        const unsigned wdo = WDO();
        const char* wob = (const char*)wo + (size_t)wid * 64 * 1024 + nb * 128;
#pragma unroll
        for (int e = 0; e < 8; ++e) dma16(wdo, wob + e * 8192, lds0 + wid * 8192 + e * 1024);
    }
    if (blk == 0 && wid == 0) {
        const int lane = fresh_lane();
#pragma unroll 1
        for (int i = 0; i < 4; ++i) {
            const int w = i * 64 + lane;
            flag_wait<8>(ws, WS_KVF + (unsigned)(((w >> 4) * 4) * 16 + (w & 15)) * 8, tag);
            const int cj = w & 3;
            flag_wait<8>(ws, WS_HF + (unsigned)((2 * (w >> 4)) * 64 + ((w >> 2) & 3) * 14 + (cj < 2 ? 3 * cj : 4 * cj - 2)) * 8, tag);
        }
        if (lane == 0) flag_set(ws, WS_E, tag);
    }
    STAMP(1, 5);
    if (wid == 0) flag_wait<2, 24>(ws, WS_OF + (unsigned)(mb * 64 + fresh_lane()) * 8, tag);
    WAIT_VM_BAR(0);
    STAMP(1, 6);
    {
        LANE16();
        const int tt = wid & 3, ch = wid >> 2;
        const int tt16 = mb * 4 + tt;
        const int rd0 = (16 * (g4 >> 1) + 4 * (g4 & 1) + q4) * 128 + (((2 * ch) ^ ((q4 & 2) | (g4 & 1))) << 5) + p4 * 8;
        u32x4 of[16];
#pragma unroll
        for (int k = 0; k < 16; ++k) of[k] = ld16_dev(rs, (unsigned)(lane * 16), WS_O + (unsigned)(tt16 * 16 + k) * 1024);
        const float gt = __uint_as_float(__builtin_amdgcn_raw_buffer_load_b32(rs, (unsigned)(r16 * 4), WS_G + (unsigned)tt16 * 64, 16));
        f32x4 a0 = {0.f, 0.f, 0.f, 0.f}, a1 = a0;
#pragma unroll
        for (int k = 0; k < 16; ++k) {
            const bf16x8 w0 = cat8(tr_read(L + rd0 + k * 4096), tr_read(L + rd0 + k * 4096 + 1024));
            const bf16x8 w1 = cat8(tr_read(L + (rd0 ^ 32) + k * 4096), tr_read(L + (rd0 ^ 32) + k * 4096 + 1024));
            a0 = MFMA16(w0, BC8(of[k]), a0); a1 = MFMA16(w1, BC8(of[k]), a1);
        }
        const float gb = 1.f - gt;
        const f32x4 r0 = a0 * gt + bsum[0] * gb, r1 = a1 * gt + bsum[1] * gb;
        const auto s0 = __builtin_amdgcn_permlane16_swap(cvtpk(r0[0], r0[1]), cvtpk(r1[0], r1[1]), false, false);
        const auto s1 = __builtin_amdgcn_permlane16_swap(cvtpk(r0[2], r0[3]), cvtpk(r1[2], r1[3]), false, false);
        const u32x4 ov = {s0[0], s1[0], s0[1], s1[1]};
        u16* op = out + (size_t)(tt16 * 16 + r16) * DM + nb * 64 + ch * 32 + (g4 & 1) * 16 + (g4 >> 1) * 8;
        *(u32x4*)op = ov;
    }
    STAMP(1, 7);
}

__global__ __launch_bounds__(512, 4) void fused_fwd(const u16* __restrict__ x, const u16* __restrict__ wqkv, const u16* __restrict__ wo,
                                                     const u16* __restrict__ wg, const u16* __restrict__ wu, const u16* __restrict__ wd,
                                                     const u16* __restrict__ gw, char* __restrict__ ws, u16* __restrict__ out) {
    extern __shared__ __attribute__((aligned(16))) char lds[];
    if (blockIdx.x < 256) role_a(blockIdx.x, x, wqkv, gw, ws, lds);
    else role_f(blockIdx.x - 256, x, wo, wg, wu, wd, ws, out, lds);
}

extern "C" void kernel_launch(void* const* d_in, const int* in_sizes, int n_in, void* d_out, int out_size, void* d_ws, size_t ws_size,
                              hipStream_t stream) {
    (void)hipFuncSetAttribute((const void*)fused_fwd, hipFuncAttributeMaxDynamicSharedMemorySize, LDS_BYTES);
    fused_fwd<<<512, 512, LDS_BYTES, stream>>>((const u16*)d_in[0], (const u16*)d_in[1], (const u16*)d_in[2], (const u16*)d_in[4],
                                               (const u16*)d_in[5], (const u16*)d_in[6], (const u16*)d_in[7], (char*)d_ws, (u16*)d_out);
}
```

```cpp
#include <hip/hip_runtime.h>
#include <hip/hip_bf16.h>
#include <stdint.h>

typedef short bf16x8 __attribute__((ext_vector_type(8)));
typedef short s16x4 __attribute__((ext_vector_type(4)));
typedef float f32x16 __attribute__((ext_vector_type(16)));
typedef float f32x4 __attribute__((ext_vector_type(4)));
typedef float f32x2 __attribute__((ext_vector_type(2)));
typedef unsigned u32x4 __attribute__((ext_vector_type(4)));
typedef unsigned u32x2 __attribute__((ext_vector_type(2)));
typedef __bf16 bf16x2_t __attribute__((ext_vector_type(2)));
typedef unsigned short u16;
typedef unsigned long long u64;
#define LAS __attribute__((address_space(3)))
#define GAS __attribute__((address_space(1)))
#define DI __device__ __forceinline__
#define MFMA32(a, b, c) __builtin_amdgcn_mfma_f32_32x32x16_bf16((a), (b), (c), 0, 0, 0)
#define MFMA16(a, b, c) __builtin_amdgcn_mfma_f32_16x16x32_bf16((a), (b), (c), 0, 0, 0)
#define SBAR() __builtin_amdgcn_sched_barrier(0)
#define PIN(x) asm volatile("" : "+v"(x))
#define BC8(x) __builtin_bit_cast(bf16x8, x)
template <int V> struct IC { static constexpr int value = V; };
template <int I, int N, class F> DI void static_for(F&& f) { if constexpr (I < N) { f(IC<I>{}); static_for<I + 1, N>(f); } }

constexpr int SEQ = 2048, DM = 512, FF = 1792;
constexpr unsigned WS_E = 0;
constexpr unsigned WS_KVF = 4096;
constexpr unsigned WS_HF = WS_KVF + 8192;
constexpr unsigned WS_OF = WS_HF + 16384;
constexpr unsigned WS_G = WS_OF + 16384;
constexpr unsigned WS_KV = 65536;
constexpr unsigned WS_H = WS_KV + (4u << 20);
constexpr unsigned WS_O = WS_H + 128u * 56 * 1024;
constexpr unsigned WS_END = WS_O + 128u * 16 * 1024;
constexpr float QSCALE = 0.25f * 1.4426950408889634f;
constexpr int LDS_BYTES = 81920;
constexpr unsigned EMPTY = 0xffffffffu;

DI unsigned cvtpk(float lo, float hi) { const f32x2 v = {lo, hi}; return __builtin_bit_cast(unsigned, __builtin_convertvector(v, bf16x2_t)); }
DI s16x4 tr_read(LAS const char* p) { return __builtin_amdgcn_ds_read_tr16_b64_v4i16((LAS s16x4*)p); }
DI bf16x8 cat8(s16x4 a, s16x4 b) { return __builtin_shufflevector(a, b, 0, 1, 2, 3, 4, 5, 6, 7); }
DI void dma16(unsigned voff, const void* sbase, unsigned lds) {
    asm volatile("s_mov_b32 m0, %2\n\ts_nop 0\n\tglobal_load_lds_dwordx4 %0, %1" :: "v"(voff), "s"(sbase), "s"(lds) : "memory");
}
DI void dma16_dev(unsigned voff, const void* sbase, unsigned lds) {
    asm volatile("s_mov_b32 m0, %2\n\ts_nop 0\n\tglobal_load_lds_dwordx4 %0, %1 sc1" :: "v"(voff), "s"(sbase), "s"(lds) : "memory");
}
DI void dma16_48(unsigned voff, const void* sbase, unsigned lds) {
    asm volatile("s_mov_b32 m0, %2\n\ts_mov_b32 exec_hi, 0xffff\n\tglobal_load_lds_dwordx4 %0, %1\n\ts_mov_b32 exec_hi, -1" :: "v"(voff), "s"(sbase), "s"(lds) : "memory");
}
DI void touch(unsigned voff, const void* sbase, unsigned lds) {
    asm volatile("s_mov_b32 m0, %2\n\ts_nop 0\n\tglobal_load_lds_dword %0, %1" :: "v"(voff), "s"(sbase), "s"(lds) : "memory");
}
template <int N, int STEP> DI void stouch(const void* a, const void* b) {
    unsigned d;
    asm volatile(".set .Lso_, 0\n\t.rept %3\n\ts_load_dword %0, %1, .Lso_\n\ts_load_dword %0, %2, .Lso_\n\t.set .Lso_, .Lso_+%4\n\t.endr\n\ts_waitcnt lgkmcnt(0)"
                 : "=&s"(d) : "s"(a), "s"(b), "n"(N), "n"(STEP) : "memory");
}
template <int N> DI void wait_vm_bar() { asm volatile("s_waitcnt vmcnt(%0) lgkmcnt(0)\n\ts_barrier" :: "n"(N) : "memory"); }
#define WAIT_VM(N) asm volatile("s_waitcnt vmcnt(" #N ")" ::: "memory")
#define WAIT_VM_BAR(N) asm volatile("s_waitcnt vmcnt(" #N ") lgkmcnt(0)\n\ts_barrier" ::: "memory")
#define LGKM_BAR() asm volatile("s_waitcnt lgkmcnt(0)\n\ts_barrier" ::: "memory")
#define BAR() asm volatile("s_barrier" ::: "memory")
typedef __amdgpu_buffer_rsrc_t rsrc_t;
DI void st16_dev(rsrc_t r, unsigned off, u32x4 v) { __builtin_amdgcn_raw_buffer_store_b128(v, r, off, 0, 16); }
DI u32x4 ld16_dev(rsrc_t r, unsigned voff, unsigned soff) { return __builtin_amdgcn_raw_buffer_load_b128(r, voff, soff, 16); }
DI void flag_set(char* ws, unsigned off, u64 tag) { __hip_atomic_store((GAS u64*)(ws + off), tag, __ATOMIC_RELAXED, __HIP_MEMORY_SCOPE_AGENT); }
DI u64 flag_get(const char* ws, unsigned off) { return __hip_atomic_load((const GAS u64*)(ws + off), __ATOMIC_RELAXED, __HIP_MEMORY_SCOPE_AGENT); }
template <int NAP, int LONG_NAP = NAP> DI void flag_wait(const char* ws, unsigned off, u64 tag) {
    const u64 t0 = __builtin_amdgcn_s_memrealtime();
    for (;;) {
        const u64 v = __hip_atomic_load((const GAS u64*)(ws + off), __ATOMIC_RELAXED, __HIP_MEMORY_SCOPE_AGENT);
        const u64 got = __ballot(v == tag);
        if (got == ~0ull) break;
        if (__builtin_amdgcn_s_memrealtime() - t0 > 2000000ull) break;
        if (LONG_NAP != NAP && got == 0) __builtin_amdgcn_s_sleep(LONG_NAP); else __builtin_amdgcn_s_sleep(NAP);
    }
    asm volatile("" ::: "memory");
}
template <int NAP> DI u64 flag_wait_some(const char* ws, unsigned off, u64 tag, u64 want) {
    const u64 t0 = __builtin_amdgcn_s_memrealtime();
    u64 got;
    for (;;) {
        got = __ballot(flag_get(ws, off) == tag);
        if ((got & want) == want || __builtin_amdgcn_s_memrealtime() - t0 > 2000000ull) break;
        __builtin_amdgcn_s_sleep(NAP);
    }
    asm volatile("" ::: "memory");
    return got;
}
DI u64 read_tag(const char* ws) {
    u64 e;
    asm volatile("s_load_dwordx2 %0, %1, 0x0\n\ts_waitcnt lgkmcnt(0)" : "=s"(e) : "s"(ws) : "memory");
    return e * 6364136223846793005ull + 1442695040888963407ull;
}

DI int fresh_lane() { int l; asm volatile("v_mbcnt_lo_u32_b32 %0, -1, 0\n\tv_mbcnt_hi_u32_b32 %0, -1, %0" : "=v"(l)); return l; }
#define LANE16() const int lane = fresh_lane(), r16 = lane & 15, g4 = lane >> 4, q4 = r16 >> 2, p4 = lane & 3; (void)r16; (void)g4; (void)q4; (void)p4

#define STAMP(k, i) do { } while (0)
#define STAMP3(k, i) do { } while (0)
#define LSTAMP(j) do { } while (0)
#define LDECL() do { } while (0)
#define LFLUSH(k) do { } while (0)

#define KV_POLLER 0
#define PF2_N 0
#define KV_NAP 3
constexpr int P_W = 49152;
constexpr int P_GW = P_W + 5 * 6144;
constexpr int A_SLOT = 17408;
constexpr int A_Q = 4 * A_SLOT;

DI f32x16 attn_exact(rsrc_t rs, unsigned kvbase, bf16x8 qf, int lane) {
    const int r32 = lane & 31, hh = lane >> 5;
    f32x16 o, zero;
#pragma unroll
    for (int i = 0; i < 16; ++i) { o[i] = 0.f; zero[i] = 0.f; }
    float m = -INFINITY;
    const unsigned cst = ((r32 & 27) == 16) ? 0x3f803f80u : 0u;
    const u32x4 cv = {cst, cst, cst, cst};
#pragma unroll 1
    for (int t = 0; t < 64; ++t) {
        const unsigned tb = kvbase + (t >> 2) * 16384 + ((t >> 1) & 1) * 4096;
        const bf16x8 kf = BC8(ld16_dev(rs, tb + (t & 1) * 1024 + lane * 16, 0));
        const f32x16 s = MFMA32(kf, qf, zero);
        float tm = s[0];
#pragma unroll
        for (int i = 1; i < 16; ++i) tm = fmaxf(tm, s[i]);
        tm = fmaxf(tm, __shfl_xor(tm, 32));
        const float mn = fmaxf(m, tm);
        const float alpha = __builtin_amdgcn_exp2f(m - mn);
        m = mn;
#pragma unroll
        for (int i = 0; i < 16; ++i) o[i] *= alpha;
#pragma unroll
        for (int st = 0; st < 2; ++st) {
            u32x4 pw;
#pragma unroll
            for (int j = 0; j < 4; ++j) pw[j] = cvtpk(__builtin_amdgcn_exp2f(s[8 * st + 2 * j] - m), __builtin_amdgcn_exp2f(s[8 * st + 2 * j + 1] - m));
            const u32x4 vv = ld16_dev(rs, tb + 8192 + hh * 1024 + ((t & 1) * 2 + st) * 256 + (r32 & 15) * 16, 0);
            o = MFMA32(BC8(r32 < 16 ? vv : cv), BC8(pw), o);
        }
    }
    return o;
}

DI void role_a(int blk, const u16* __restrict__ x, const u16* __restrict__ wqkv, const u16* __restrict__ gw,
               const u16* __restrict__ wd, const u16* __restrict__ wo, char* __restrict__ ws, char* lds, const char* code) {
    const int lane = fresh_lane(), wid = __builtin_amdgcn_readfirstlane(threadIdx.x >> 6);
    const int r32 = lane & 31, hh = lane >> 5;
    const int xcd = blk & 7, bi = blk >> 3;
    const int qb = (xcd & 1) * 8 + (bi & 7), hp = (xcd >> 1) * 4 + (bi >> 3);
    const unsigned lds0 = (unsigned)(uintptr_t)lds;
    LAS char* L = (LAS char*)lds;
    const rsrc_t rs = __builtin_amdgcn_make_buffer_rsrc(ws, 0, WS_END, 0x00020000);
    const unsigned kvown = WS_KV + (unsigned)(hp * 16 + qb) * 16384;
    u64 tag;
    STAMP(0, 0);
    __builtin_amdgcn_s_setprio(3);
#define LD8(A) (*(LAS const bf16x8*)(L + (A)))
    {
        const int lw = 2 * (wid & 3);
        const int xrow = 8 * lw + (lane >> 3);
        const unsigned xo = (unsigned)(xrow * 1024 + (((lane & 7) ^ ((xrow >> 1) & 7)) << 4)), xo2 = (xo ^ 64) + 8192;
        const unsigned wo_ = (unsigned)((4 * lw + ((lane >> 2) & 3)) * 3072 + (lane >> 4) * 1024 + (lane & 3) * 16);
        const char* xb = (const char*)x + (size_t)qb * 128 * 1024;
        const char* wb = (const char*)wqkv + hp * 64;
        auto ld_x = [&](int h) {
            const char* b = xb + (h & 1) * 65536 + (h >> 1) * 128;
            const unsigned d = lds0 + (((h >> 1) + 1) % 3) * 16384 + (h & 1) * 8192 + lw * 1024;
            dma16(xo, b, d); dma16(xo2, b, d + 1024);
        };
        auto ld_w = [&](int j) {
            const char* b = wb + (size_t)j * (32 * 3072);
            const unsigned d = lds0 + P_W + (j % 5) * 6144 + lw * 768;
            dma16_48(wo_, b, d); dma16_48(wo_, b + 4 * 3072, d + 768);
        };
        if (wid >= 4) {
#pragma unroll
            for (int j = 0; j < 5; ++j) { ld_x(j); ld_w(j); }
            ld_x(5);
        } else {
            if (wid == 3) dma16((unsigned)(lane * 16), gw, lds0 + P_GW);
#pragma unroll
            for (int e = 0; e < 2; ++e)
                st16_dev(rs, WS_O + (unsigned)((8 * qb + 2 * wid + e) * 16 + hp) * 1024 + lane * 16, (u32x4){EMPTY, EMPTY, EMPTY, EMPTY});
            if (bi < 4) touch((unsigned)(((bi * 4 + wid) * 16 + (lane & 15)) * 128), code, lds0 + P_GW + 1024 + wid * 256);
        }
        tag = read_tag(ws);
        auto proj = [&](auto KIND) {
            constexpr int kind = decltype(KIND)::value, NT = kind == 0 ? 1 : 2;
            const int row0 = kind == 0 ? (wid - 4) * 32 : (wid & 1) * 64;
            int xa[4];
#pragma unroll
            for (int k = 0; k < 4; ++k) { xa[k] = ((row0 + r32) * 128 + ((hh ^ ((r32 >> 1) & 7)) << 4)) ^ (k << 5); PIN(xa[k]); }
            const int wa = P_W + hh * 1536 + kind * 256 + ((lane & 15) >> 2) * 64 + ((lane >> 4) & 1) * 32 + (lane & 3) * 8;
            int ga = (8 * hp + (lane >> 3)) * 128 + (((lane & 7) ^ ((4 * hp + (lane >> 4)) & 7)) << 4), gb = P_GW + (lane & 7) * 16;
            if (kind == 2) { PIN(ga); PIN(gb); }
            f32x16 acc[NT];
#pragma unroll
            for (int t = 0; t < NT; ++t)
#pragma unroll
                for (int i = 0; i < 16; ++i) acc[t][i] = 0.f;
            bf16x8 xf[2][2][NT], wf[2][2];
            u32x4 gx, gv;
            float z = 0.f;
            LDECL();
            static_for<0, 17>([&](auto J) {
                constexpr int j = decltype(J)::value, c = j & 1;
                constexpr int xs = (((j >> 1) + 1) % 3) * 16384, wsl = (j % 5) * 6144;
                if constexpr (j < 16) {
                    constexpr int issued = j < 2 ? 11 : (2 * j + 8 < 32 ? 2 * j + 8 : 32);
                    wait_vm_bar<kind == 0 ? 2 * (issued - 1 - (c ? 2 * j + 1 : 2 * j + 2)) : 0>();
                    LSTAMP(j);
                }
                auto mm = [&](int ks, int t) {
                    if constexpr (j > 0) acc[t] = kind == 2 ? MFMA32(xf[c ^ 1][ks][t], wf[c ^ 1][ks], acc[t]) : MFMA32(wf[c ^ 1][ks], xf[c ^ 1][ks][t], acc[t]);
                    SBAR();
                };
                auto rw = [&](int ks) { if constexpr (j < 16) wf[c][ks] = cat8(tr_read(L + wa + wsl + ks * 3072), tr_read(L + wa + wsl + ks * 3072 + 768)); };
                auto rx = [&](int ks, int t) { if constexpr (j < 16) xf[c][ks][t] = LD8(xa[2 * c + ks] + xs + t * 4096); };
                if constexpr (kind == 0) {
                    rw(0); rx(0, 0); rw(1); rx(1, 0);
                    SBAR();
                    if constexpr (j + 4 < 16 && j >= 2) ld_x(j + 4);
                    mm(0, 0);
                    if constexpr (j + 4 < 16 && j >= 1) ld_w(j + 4);
                    mm(1, 0);
                } else {
                    mm(0, 0); rw(0); rx(0, 0); SBAR();
                    mm(0, 1); rx(0, 1); rw(1); SBAR();
                    mm(1, 0); rx(1, 0); rx(1, 1);
                    if (kind == 2 && j < 16 && c == 0 && wid == 3) { gx = *(LAS const u32x4*)(L + ga + xs); gv = *(LAS const u32x4*)(L + gb + (j >> 1) * 128); }
                    SBAR();
                    mm(1, 1);
                    if (kind == 2 && c == 1 && wid == 3) {
#pragma unroll
                        for (int e = 0; e < 4; ++e) {
                            z = fmaf(__uint_as_float(gx[e] << 16), __uint_as_float(gv[e] << 16), z);
                            z = fmaf(__uint_as_float(gx[e] & 0xffff0000u), __uint_as_float(gv[e] & 0xffff0000u), z);
                        }
                    }
                }
            });
#pragma unroll
            for (int i = 0; i < NT; ++i)
#pragma unroll
                for (int e = 0; e < 2; ++e) {
                    u32x4 v;
#pragma unroll
                    for (int j = 0; j < 4; ++j)
                        v[j] = kind == 0 ? cvtpk(acc[i][8 * e + 2 * j] * QSCALE, acc[i][8 * e + 2 * j + 1] * QSCALE) : cvtpk(acc[i][8 * e + 2 * j], acc[i][8 * e + 2 * j + 1]);
                    if constexpr (kind == 0) *(LAS u32x4*)(L + A_Q + ((wid - 4) * 2 + e) * 1024 + lane * 16) = v;
                    else {
                        const int tg = wid & 1;
                        const int off = kind == 1 ? tg * 4096 + e * 2048 + i * 1024 + lane * 16
                                                  : 8192 + tg * 4096 + (r32 >> 4) * 2048 + hh * 1024 + (2 * i + e) * 256 + (r32 & 15) * 16;
                        st16_dev(rs, kvown + off, v);
                        *(LAS u32x4*)(L + off) = v;
                    }
                }
            if (kind == 1) LFLUSH(3);
            if (kind == 2 && wid == 3) {
                z += __shfl_xor(z, 1); z += __shfl_xor(z, 2); z += __shfl_xor(z, 4);
                if ((lane & 7) == 0) __builtin_amdgcn_raw_buffer_store_b32(__float_as_uint(1.f / (1.f + __expf(-z))), rs, WS_G + (unsigned)(qb * 128 + 8 * hp + (lane >> 3)) * 4, 0, 16);
            }
        };
        if (wid >= 4) proj(IC<0>{}); else if (wid < 2) proj(IC<1>{}); else proj(IC<2>{});
    }
    const unsigned cst = ((lane & 11) == 0) ? 0x3f803f80u : 0u;
    if (wid == 4) *(LAS u32x4*)(L + 16384 + lane * 16) = (u32x4){cst, cst, cst, cst};
    LGKM_BAR();
    STAMP(0, 1);
    if (wid > 4) *(LAS u32x4*)(L + (wid - 4) * A_SLOT + 16384 + lane * 16) = (u32x4){cst, cst, cst, cst};
    const int hsel = wid & 1, qp = (wid >> 1) & 1, kh = wid >> 2;
    const bf16x8 qA = *(LAS const bf16x8*)(L + A_Q + ((2 * qp) * 2 + hsel) * 1024 + lane * 16);
    const bf16x8 qB = *(LAS const bf16x8*)(L + A_Q + ((2 * qp + 1) * 2 + hsel) * 1024 + lane * 16);
    int ka = kh * 4096 + hsel * 2048 + lane * 16;
    int va = r32 < 16 ? 8192 + kh * 4096 + hsel * 2048 + hh * 1024 + r32 * 16 : 16384 + (r32 & 15) * 16;
    PIN(ka); PIN(va);
    bf16x8 kf = LD8(ka);
    f32x16 negm, oA, oB, sX, sY;
    float mref;
    {
        f32x16 zero;
#pragma unroll
        for (int i = 0; i < 16; ++i) { zero[i] = 0.f; oA[i] = 0.f; oB[i] = 0.f; }
        sX = MFMA32(kf, qA, zero); sY = MFMA32(kf, qB, zero);
        float tm = fmaxf(sX[0], sY[0]);
#pragma unroll
        for (int i = 1; i < 16; ++i) tm = fmaxf(tm, fmaxf(sX[i], sY[i]));
        mref = fmaxf(tm, __shfl_xor(tm, 32));
#pragma unroll
        for (int i = 0; i < 16; ++i) negm[i] = -mref;
        PIN(negm);
    }
    sX = MFMA32(kf, qA, negm);
    bf16x8 v0 = LD8(va), v1 = v0;
    u32x4 p0, p1 = {0u, 0u, 0u, 0u};
    if (wid < 4) {
        WAIT_VM(0);
        if (lane == 0) flag_set(ws, WS_KVF + (unsigned)((hp * 4 + wid) * 16 + qb) * 8, tag);
    }
    __builtin_amdgcn_s_setprio(0);
    const char* kvg = ws + WS_KV + (size_t)hp * 16 * 16384 + wid * 2048;
    const unsigned doff = (unsigned)(lane * 16);
    auto fetch = [&](int c, int slot) {
        const char* b = kvg + (size_t)((qb + c) & 15) * 16384;
        const unsigned d = lds0 + slot * A_SLOT + wid * 2048;
        dma16_dev(doff, b, d); dma16_dev(doff, b + 1024, d + 1024);
    };
    auto o_slot = [&](int l) { return WS_O + (unsigned)((8 * qb + 4 * qp + 2 * kh + ((l >> 4) & 1)) * 16 + hp) * 1024 + ((2 * hsel + (l >> 5)) * 16 + (l & 15)) * 16; };
#define EX2(X, i) X[i] = __builtin_amdgcn_exp2f(X[i])
#define HALF(SC, M1, L1, M2, L2, M3, L3) do { \
        EX2(SC, 0); EX2(SC, 1); PIN(SC); SBAR(); \
        M1; SBAR(); L1; \
        EX2(SC, 2); EX2(SC, 3); EX2(SC, 4); EX2(SC, 5); EX2(SC, 6); EX2(SC, 7); PIN(SC); SBAR(); \
        M2; SBAR(); L2; \
        p0[0] = cvtpk(SC[0], SC[1]); p0[1] = cvtpk(SC[2], SC[3]); p0[2] = cvtpk(SC[4], SC[5]); p0[3] = cvtpk(SC[6], SC[7]); PIN(p0); SBAR(); \
        EX2(SC, 8); EX2(SC, 9); EX2(SC, 10); EX2(SC, 11); PIN(SC); SBAR(); \
        M3; SBAR(); L3; \
        EX2(SC, 12); EX2(SC, 13); EX2(SC, 14); EX2(SC, 15); \
        p1[0] = cvtpk(SC[8], SC[9]); p1[1] = cvtpk(SC[10], SC[11]); p1[2] = cvtpk(SC[12], SC[13]); p1[3] = cvtpk(SC[14], SC[15]); PIN(p1); SBAR(); \
    } while (0)
#define CHUNK(S0, S1, SYNC) do { \
        HALF(sX, oB = MFMA32(v1, BC8(p1), oB), v1 = LD8(va + (S0) + 256), sY = MFMA32(kf, qB, negm), kf = LD8(ka + (S0) + 1024), oA = MFMA32(v0, BC8(p0), oA), (void)0); \
        HALF(sY, oA = MFMA32(v1, BC8(p1), oA), (void)0, sX = MFMA32(kf, qA, negm), (void)0, oB = MFMA32(v0, BC8(p0), oB), v0 = LD8(va + (S0) + 512)); \
        HALF(sX, oB = MFMA32(v1, BC8(p1), oB), v1 = LD8(va + (S0) + 768), sY = MFMA32(kf, qB, negm), SYNC; kf = LD8(ka + (S1)), oA = MFMA32(v0, BC8(p0), oA), (void)0); \
        HALF(sY, oA = MFMA32(v1, BC8(p1), oA), (void)0, sX = MFMA32(kf, qA, negm), (void)0, oB = MFMA32(v0, BC8(p0), oB), v0 = LD8(va + (S1))); \
    } while (0)
#pragma unroll 1
    for (int n = 0; n < 4; ++n) {
        auto sync_a = [&]() {
            if (n == 0) {
                STAMP(0, 2);
                if (wid == KV_POLLER) flag_wait<KV_NAP>(ws, WS_KVF + (unsigned)(hp * 64 + fresh_lane()) * 8, tag);
                BAR();
                STAMP(0, 3);
                fetch(1, 1); fetch(2, 2);
            }
            STAMP3(2, 4 * n);
            WAIT_VM_BAR(0);
            STAMP3(2, 4 * n + 1);
            if (n == 0) STAMP(0, 4);
            fetch(4 * n + 3, 3);
            if (n < 3) fetch(4 * n + 4, 0);
        };
        auto sync_b = [&]() {
            STAMP3(2, 4 * n + 2);
            WAIT_VM_BAR(0);
            STAMP3(2, 4 * n + 3);
            if (n < 3) { fetch(4 * n + 5, 1); fetch(4 * n + 6, 2); }
            if (n == PF2_N) {
                const int l = fresh_lane();
                touch((unsigned)((l < 56 ? l : 55) * 128), (const char*)wd + (size_t)(bi * 8 + wid) * 7168, lds0 + A_Q + wid * 256);
                touch((unsigned)((l & 15) * 128), (const char*)wo + (size_t)(bi * 8 + wid) * 2048, lds0 + A_Q + wid * 256);
            }
        };
        CHUNK(0, A_SLOT, sync_a());
        CHUNK(A_SLOT, 2 * A_SLOT, (void)0);
        CHUNK(2 * A_SLOT, 3 * A_SLOT, sync_b());
        CHUNK(3 * A_SLOT, 0, (void)0);
    }
    oB = MFMA32(v1, BC8(p1), oB);
    STAMP(0, 5);
    {
        LAS float* ex = (LAS float*)(L + A_SLOT + wid * 2560) + lane;
#pragma unroll
        for (int i = 0; i < 9; ++i) ex[i * 64] = kh ? oA[i] : oB[i];
        ex[9 * 64] = mref;
    }
    LGKM_BAR();
    f32x16 o;
    {
        LAS const float* ex = (LAS const float*)(L + A_SLOT + (wid ^ 4) * 2560) + lane;
        const float mo = ex[9 * 64], mm = fmaxf(mref, mo);
        const float f1 = __builtin_amdgcn_exp2f(mref - mm), f2 = __builtin_amdgcn_exp2f(mo - mm);
#pragma unroll
        for (int i = 0; i < 9; ++i) o[i] = (kh ? oB[i] : oA[i]) * f1 + ex[i * 64] * f2;
        bool ok = (kh ? oB[8] : oA[8]) < 1.0995116e12f && ex[8 * 64] < 1.0995116e12f && o[8] > 9.0949470e-13f;
#pragma unroll
        for (int i = 0; i < 8; ++i) ok = ok && (fabsf(o[i]) < 3.0e38f);
        if (__builtin_expect(!__all(ok), 0)) o = attn_exact(rs, WS_KV + (unsigned)hp * 16 * 16384 + hsel * 2048, kh ? qB : qA, lane);
    }
    {
        const float inv = 1.f / o[8];
        u32x4 w;
#pragma unroll
        for (int j = 0; j < 4; ++j) w[j] = min(cvtpk(o[2 * j] * inv, o[2 * j + 1] * inv), EMPTY - 1);
        st16_dev(rs, o_slot(lane), w);
    }
    BAR();
    if (wid == 0 && lane == 0) flag_set(ws, WS_OF + (unsigned)(qb * 16 + hp) * 8, tag);
    STAMP(0, 6);
}

#define F_DELAYA 4
#define F_PRIO_DN 0
#define F_DT 40
#define PFW 2
#define O_NAP 12
#define O_NAP2 3
#define O_PRIO 0
#define O_DIRECT_N 2
#define F_T0 30
#define F_T1 500
#define F_PRIO 2
#define F_HI() __builtin_amdgcn_s_setprio(F_PRIO)
#define F_LO() __builtin_amdgcn_s_setprio(0)
DI void role_f(int blk, const u16* __restrict__ x, const u16* __restrict__ wqkv, const u16* __restrict__ wo, const u16* __restrict__ wg, const u16* __restrict__ wu,
               const u16* __restrict__ wd, char* __restrict__ ws, u16* __restrict__ out, char* lds) {
    const int wid = __builtin_amdgcn_readfirstlane(threadIdx.x >> 6);
    const int xcd = blk & 7, bi = blk >> 3;
    const int cq = xcd >> 1, rb = (xcd & 1) * 8 + (bi & 7), cb = cq * 4 + (bi >> 3);
    const int mb = 4 * xcd + (bi >> 3), nb = bi & 7;
    const unsigned lds0 = (unsigned)(uintptr_t)lds;
    LAS char* L = (LAS char*)lds;
    const rsrc_t rs = __builtin_amdgcn_make_buffer_rsrc(ws, 0, WS_END, 0x00020000);
    STAMP(1, 0);
    const u64 tstart = __builtin_amdgcn_s_memrealtime();
    auto wait_until = [&](unsigned ticks) { while ((unsigned)(__builtin_amdgcn_s_memrealtime() - tstart) < ticks) __builtin_amdgcn_s_sleep(2); };
    F_HI();
    {
        const int lane = fresh_lane(), idx = bi * 8 + wid;
        __builtin_amdgcn_s_sleep(F_DELAYA);
        const int xl = lane < 20 ? lane : 19;
        touch((unsigned)(idx * 4096 + (xl & 3) * 1024 + (3 + (xl >> 2)) * 128), (const char*)x + (size_t)(xcd & 1) * (1 << 20), lds0 + wid * 1024);
        const int q = lane < 12 ? lane : 11, qr = q >= 6, qc = q - 6 * qr;
        if (idx >= 80) touch((unsigned)((2 * idx + qr) * 3072 + (qc >> 1) * 1024 + (qc & 1) * 128), (const char*)wqkv + cq * 256, lds0 + wid * 1024);
    }
    const u64 tag = read_tag(ws);
    {
        LANE16();
        const int r32 = lane & 31, hh = lane >> 5;
        const int xrow = 8 * wid + (lane >> 3);
        unsigned xo = (unsigned)(xrow * 1024 + (((lane & 7) ^ ((xrow >> 1) & 7)) << 4));
        const int bg = (r16 >> 1) ^ (2 * g4), bu = bg ^ 1;
        const unsigned wrow = (unsigned)((4 * wid + g4) * (FF * 2) + (lane & 1) * 16);
        unsigned go = wrow + (bg < 7 ? bg : 6) * 32, uo = wrow + (bu < 7 ? bu : 6) * 32;
        PIN(xo); PIN(go); PIN(uo);
        const char* xb = (const char*)x + (size_t)rb * 128 * 1024;
        const char* gb = (const char*)wg + cb * 224;
        const char* ub = (const char*)wu + cb * 224;
        const unsigned dw = lds0 + wid * 1024;
        auto ld_x = [&](int s) { const unsigned d = dw + (s & 1) * 16384; dma16(xo, xb + s * 128, d); dma16(xo, xb + 65536 + s * 128, d + 8192); };
        auto ld_w = [&](int j) { const unsigned d = dw + 32768 + (j % 3) * 16384; const size_t o = (size_t)j * (32 * FF * 2); dma16(go, gb + o, d); dma16(uo, ub + o, d + 8192); };
        auto gu = [&](auto KIND) {
            constexpr int NT = decltype(KIND)::value, NU = 4 - NT;
            const int t0 = NT == 2 ? (wid & 1) * 2 : wid - 4, u0 = NT == 2 ? (wid >> 1) * 2 : 4;
            int xa[4], wa[NU];
#pragma unroll
            for (int k = 0; k < 4; ++k) { xa[k] = ((t0 * 32 + r32) * 128 + ((hh ^ ((r32 >> 1) & 7)) << 4)) ^ (k << 5); PIN(xa[k]); }
#pragma unroll
            for (int u = 0; u < NU; ++u) { wa[u] = 32768 + (g4 & 1) * 8192 + (8 * hh + q4) * 256 + (((u0 + u) ^ (2 * q4 + (g4 & 1))) << 5) + p4 * 8; PIN(wa[u]); }
            f32x16 acc[NT][NU];
#pragma unroll
            for (int t = 0; t < NT; ++t)
#pragma unroll
                for (int u = 0; u < NU; ++u)
#pragma unroll
                    for (int i = 0; i < 16; ++i) acc[t][u][i] = 0.f;
            bf16x8 x0[NT], w0[NU], x1[2][NT], w1[2][NU];
            auto rd = [&](auto J, int ks, bf16x8 (&xf)[NT], bf16x8 (&wf)[NU]) {
                constexpr int j = decltype(J)::value;
#pragma unroll
                for (int t = 0; t < NT; ++t) xf[t] = LD8(xa[2 * (j & 1) + ks] + ((j >> 1) & 1) * 16384 + t * 4096);
#pragma unroll
                for (int u = 0; u < NU; ++u) { const int a = wa[u] + (j % 3) * 16384 + ks * 4096; wf[u] = cat8(tr_read(L + a), tr_read(L + a + 1024)); }
            };
            auto mm = [&](bf16x8 (&xf)[NT], bf16x8 (&wf)[NU]) {
#pragma unroll
                for (int t = 0; t < NT; ++t)
#pragma unroll
                    for (int u = 0; u < NU; ++u) acc[t][u] = MFMA32(wf[u], xf[t], acc[t][u]);
            };
            {
                const int g = lane < 14 ? lane : 13, gr = g >= 7, gc = g - 7 * gr;
                unsigned po = (unsigned)((2 * (bi * 8 + wid) + gr) * (FF * 2) + gc * 128);
                PIN(po);
                wait_until(F_T0);
                touch(po, (const char*)wg + cq * 896, dw);
                touch(po, (const char*)wu + cq * 896, dw);
                wait_until(F_T1);
            }
            ld_x(0); ld_w(0);
            BAR();
            ld_w(1); ld_x(1); ld_w(2);
            F_LO();
            LDECL();
            WAIT_VM_BAR(6);
            STAMP(1, 1);
            rd(IC<0>{}, 0, x0, w0); rd(IC<0>{}, 1, x1[0], w1[0]);
            static_for<0, 16>([&](auto J) {
                constexpr int j = decltype(J)::value, c = j & 1;
                wait_vm_bar<j >= 14 ? 0 : (c ? 2 : 4)>();
                LSTAMP(j);
                if constexpr (c == 0) { if constexpr (j + 3 < 16) ld_w(j + 3); }
                else if constexpr (j + 3 < 16) { ld_x((j + 3) >> 1); ld_w(j + 3); }
                if constexpr (j < 15) rd(IC<j + 1>{}, 1, x1[c ^ 1], w1[c ^ 1]);
                SBAR();
                mm(x0, w0);
                SBAR();
                if constexpr (j < 15) rd(IC<j + 1>{}, 0, x0, w0);
                SBAR();
                mm(x1[c], w1[c]);
                SBAR();
            });
            F_HI();
            STAMP(1, 2);
            const int lane = fresh_lane();
#pragma unroll
            for (int t = 0; t < NT; ++t)
#pragma unroll
                for (int u = 0; u < NU; ++u) {
                    u32x4 v;
#pragma unroll
                    for (int j = 0; j < 4; ++j) v[j] = cvtpk(acc[t][u][2 * j] * acc[t][u][8 + 2 * j], acc[t][u][2 * j + 1] * acc[t][u][9 + 2 * j]);
                    st16_dev(rs, WS_H + (unsigned)((rb * 4 + t0 + t) * 112 + 7 * cb + u0 + u) * 1024 + lane * 16, v);
                }
            WAIT_VM(0);
            if (NT == 2) LFLUSH(2);
            if (lane == 0) flag_set(ws, WS_HF + (unsigned)((2 * rb + (t0 >> 1)) * 64 + cb * 4 + (NT == 2 ? wid >> 1 : 2 + (wid & 1))) * 8, tag);
        };
        if (wid < 4) gu(IC<2>{}); else gu(IC<1>{});
    }
#define WDO() (unsigned)((lane >> 3) * 1024 + ((((lane & 7) >> 1) ^ (((lane >> 3) & 2) | (lane >> 5))) << 5) + (lane & 1) * 16)
    f32x4 bsum[2];
    {
        LANE16();
        const int r32 = lane & 31, hh = lane >> 5;
        const unsigned wdo = WDO(), hv = (unsigned)(lane * 16);
        const char* wdb = (const char*)wd + (size_t)wid * 8192 + nb * 128;
        const char* hb = ws + WS_H + (size_t)((2 * mb + (wid >> 2)) * 112 + (wid & 3)) * 1024;
        unsigned dd = lds0 + wid * 1024;
        asm volatile("" : "+s"(dd));
        auto ld_wd = [&](int s) { dma16(wdo, wdb + (size_t)s * 65536, dd + (s % 5) * 16384 + 8192); };
        auto ld_h = [&](int s) { dma16_dev(hv, hb + s * 4096, dd + (s % 5) * 16384); };
#pragma unroll
        for (int s = 0; s < 4; ++s) ld_wd(s);
        const int t = wid & 1, c = (wid >> 1) & 1, kh = wid >> 2;
        int ha[2], wa[2];
#pragma unroll
        for (int e = 0; e < 2; ++e) {
            ha[e] = e * 49152 + (t * 4 + 2 * kh) * 1024 + lane * 16;
            wa[e] = e * 49152 + 8192 + kh * 4096 + (4 * hh + q4) * 128 + (((2 * c + (g4 & 1)) ^ ((q4 & 2) | hh)) << 5) + p4 * 8;
            PIN(ha[e]); PIN(wa[e]);
        }
        unsigned hfo = WS_HF + (unsigned)(mb * 64 + lane) * 8;
        PIN(hfo);
        u64 seen = ~0ull;
        if (wid == 0) seen = flag_wait_some<4>(ws, hfo, tag, 0xffffull);
        const char* wob = (const char*)wo + (size_t)wid * 32768 + nb * 128;
        auto ld_wo = [&](int half) {
#pragma unroll
            for (int e = 0; e < 4; ++e) dma16(wdo, wob + half * 262144 + e * 8192, lds0 + (half ? 0 : 49152) + wid * 4096 + e * 1024);
        };
        BAR();
        STAMP(1, 3);
#pragma unroll
        for (int s = 0; s < 4; ++s) ld_h(s);
        __builtin_amdgcn_s_setprio(F_PRIO_DN);
        f32x16 acc;
#pragma unroll
        for (int i = 0; i < 16; ++i) acc[i] = 0.f;
        bf16x8 hf[2][2], wf[2][2];
        static_for<0, 29>([&](auto S) {
            constexpr int s = decltype(S)::value, p = s & 1, sl = s % 5, e = sl >= 3, so = (sl - 3 * e) * 16384;
            if constexpr (s < 28) {
                if constexpr (s % 7 == 3 && s < 24) {
                    constexpr u64 want = 0xffffull << (16 * ((s + 4) / 7));
                    if ((seen & want) != want) { F_HI(); seen = flag_wait_some<4>(ws, hfo, tag, want); __builtin_amdgcn_s_setprio(F_PRIO_DN); }
                }
                wait_vm_bar<(s < 4 ? 3 + s : (s < 25 ? 6 : (s == 25 ? 4 : 58 - 2 * s)))>();
                if constexpr (s + 4 < 28) { ld_wd(s + 4); ld_h(s + 4); }
                if constexpr (s == 25) ld_wo(0);
                if constexpr (s == 27) ld_wo(1);
#pragma unroll
                for (int k = 0; k < 2; ++k) {
                    hf[p][k] = LD8(ha[e] + so + k * 1024);
                    wf[p][k] = cat8(tr_read(L + wa[e] + so + k * 2048), tr_read(L + wa[e] + so + k * 2048 + 1024));
                }
            }
            SBAR();
            if constexpr (s > 0) { acc = MFMA32(wf[p ^ 1][0], hf[p ^ 1][0], acc); acc = MFMA32(wf[p ^ 1][1], hf[p ^ 1][1], acc); }
            SBAR();
        });
        F_HI();
        STAMP(1, 4);
        LAS f32x4* red = (LAS f32x4*)(L + 32768 + ((c * 8 + hh) * 64 + t * 32 + r32) * 16);
        BAR();
        if (kh) {
#pragma unroll
            for (int j = 0; j < 4; ++j) red[j * 128] = (f32x4){acc[4 * j], acc[4 * j + 1], acc[4 * j + 2], acc[4 * j + 3]};
        }
        LGKM_BAR();
        if (!kh) {
#pragma unroll
            for (int j = 0; j < 4; ++j) red[j * 128] += (f32x4){acc[4 * j], acc[4 * j + 1], acc[4 * j + 2], acc[4 * j + 3]};
        }
        LGKM_BAR();
        const int ro = 32768 + (((wid >> 2) * 8 + g4) * 64 + (wid & 3) * 16 + r16) * 16;
#pragma unroll
        for (int r = 0; r < 2; ++r) bsum[r] = *(LAS const f32x4*)(L + ro + r * 4096);
        PIN(bsum[0]); PIN(bsum[1]);
    }
    if (blk == 0 && wid == 7) {
        const int lane = fresh_lane();
        for (;;) {
            bool ok = true;
#pragma unroll
            for (int i = 0; i < 4; ++i) {
                const int w = i * 64 + lane;
                ok = ok & (flag_get(ws, WS_KVF + (unsigned)((w >> 4) * 64 + (w & 15)) * 8) == tag) & (flag_get(ws, WS_HF + (unsigned)((w >> 4) * 128 + (w & 15) * 4) * 8) == tag);
            }
            if (__all(ok) || __builtin_amdgcn_s_memrealtime() - tstart > 4000000ull) break;
            __builtin_amdgcn_s_sleep(8);
        }
        if (lane == 0) flag_set(ws, WS_E, tag);
    }
    if (wid == 0) flag_wait<4>(ws, WS_KVF + (unsigned)(fresh_lane() * 16 + (mb >> 1)) * 8, tag);
    WAIT_VM_BAR(0);
    STAMP(1, 5);
    {
        LANE16();
        const int tt = wid & 3, ch = wid >> 2;
        const int tt16 = mb * 4 + tt;
        int rd[4];
#pragma unroll
        for (int c = 0; c < 4; ++c) { rd[c] = (ch ? 0 : 49152) + (16 * (g4 >> 1) + 4 * (g4 & 1) + q4) * 128 + ((((2 * ch + c) & 3) ^ ((q4 & 2) | (g4 & 1))) << 5) + p4 * 8; PIN(rd[c]); }
        unsigned oo = (unsigned)((tt16 * 16 + r16) * DM + nb * 64 + ch * 32 + (g4 & 1) * 16 + (g4 >> 1) * 8) * 2;
        int xw = 32768 + wid * 2048 + lane * 16, xr = xw ^ 8192;
        PIN(oo); PIN(xw); PIN(xr);
        bf16x8 wf[4][4];
        auto rdw = [&](int k) {
#pragma unroll
            for (int c = 0; c < 4; ++c) wf[k & 3][c] = cat8(tr_read(L + rd[c] + k * 4096), tr_read(L + rd[c] + k * 4096 + 1024));
        };
#pragma unroll
        for (int k = 0; k < 4; ++k) { rdw(k); PIN(wf[k][0]); PIN(wf[k][1]); PIN(wf[k][2]); PIN(wf[k][3]); }
        BAR();
        const unsigned ov_ = (unsigned)(lane * 16), os = WS_O + (unsigned)(tt16 * 16 + ch * 8) * 1024;
        u32x4 of[8];
        unsigned gbits = 0;
#pragma unroll
        for (int k = 0; k < 8; ++k) asm volatile("" : "=v"(of[k]));
        unsigned need = 0xffu, pend = 0, up = 0;
        const u64 tw = __builtin_amdgcn_s_memrealtime();
        __builtin_amdgcn_s_setprio(O_PRIO);
        const unsigned fv = WS_OF + (unsigned)((mb >> 1) * 16 + r16) * 8, gv_ = WS_G + (unsigned)(tt16 * 16 + r16) * 4;
        asm volatile("buffer_load_dword %0, %1, %2, 0 offen sc1\n\ts_waitcnt vmcnt(0)" : "=&v"(gbits) : "v"(gv_), "s"(rs) : "memory");
        for (;;) {
            u64 v;
            unsigned so;
#define OLD(K, R) "s_bitcmp1_b32 %10, " #K "\n\ts_cbranch_scc0 1f\n\ts_add_u32 %9, %14, " #K "*1024\n\tbuffer_load_dwordx4 " R ", %11, %13, %9 offen sc1\n1:\n\t"
            asm volatile(OLD(0, "%0") OLD(1, "%1") OLD(2, "%2") OLD(3, "%3") OLD(4, "%4") OLD(5, "%5") OLD(6, "%6") OLD(7, "%7")
                         "s_bitcmp1_b32 %10, 16\n\ts_cbranch_scc0 1f\n\tbuffer_load_dwordx2 %8, %12, %13, 0 offen sc1\n1:\n\ts_waitcnt vmcnt(0)"
                         : "+v"(of[0]), "+v"(of[1]), "+v"(of[2]), "+v"(of[3]), "+v"(of[4]), "+v"(of[5]), "+v"(of[6]), "+v"(of[7]), "=&v"(v), "=&s"(so)
                         : "s"(pend | (tt == 0 ? 0x10000u : 0u)), "v"(ov_), "v"(fv), "s"(rs), "s"(os) : "memory", "scc");
            if (tt == 0) { up = (unsigned)__ballot(v == tag) >> (8 * ch); *(volatile LAS unsigned*)(L + ch * 4) = up; }
            else up = __builtin_amdgcn_readfirstlane(*(volatile LAS unsigned*)(L + ch * 4));
            if (pend) {
#pragma unroll
                for (int k = 0; k < 8; ++k)
                    if (pend >> k & 1) { if (!__any(max(max(of[k][0], of[k][1]), max(of[k][2], of[k][3])) == EMPTY)) need &= ~(1u << k); }
            }
            if (need == 0 || __builtin_amdgcn_s_memrealtime() - tw > 2000000ull) break;
            pend = __builtin_popcount(need) <= O_DIRECT_N ? need : up & need;
            if (pend == 0) { if (need == 0xffu) __builtin_amdgcn_s_sleep(O_NAP); else __builtin_amdgcn_s_sleep(O_NAP2); }
        }
        if (tt == 0) *(volatile LAS unsigned*)(L + ch * 4) = 0xffu;
        STAMP(1, 6);
        const float gt = __uint_as_float(gbits);
        f32x4 ac[4];
#pragma unroll
        for (int c = 0; c < 4; ++c) ac[c] = (f32x4){0.f, 0.f, 0.f, 0.f};
#pragma unroll
        for (int k = 0; k < 8; ++k) {
#pragma unroll
            for (int c = 0; c < 4; ++c) ac[c] = MFMA16(wf[k & 3][c], BC8(of[k]), ac[c]);
            SBAR();
            if (k + 4 < 8) rdw(k + 4);
            SBAR();
        }
        *(LAS f32x4*)(L + xw) = ac[2]; *(LAS f32x4*)(L + xw + 1024) = ac[3];
        LGKM_BAR();
        const f32x4 a0 = ac[0] + *(LAS const f32x4*)(L + xr), a1 = ac[1] + *(LAS const f32x4*)(L + xr + 1024);
        const float gb = 1.f - gt;
        const f32x4 r0 = a0 * gt + bsum[0] * gb, r1 = a1 * gt + bsum[1] * gb;
        const auto s0 = __builtin_amdgcn_permlane16_swap(cvtpk(r0[0], r0[1]), cvtpk(r1[0], r1[1]), false, false);
        const auto s1 = __builtin_amdgcn_permlane16_swap(cvtpk(r0[2], r0[3]), cvtpk(r1[2], r1[3]), false, false);
        const u32x4 ov = {s0[0], s1[0], s0[1], s1[1]};
        *(u32x4*)((char*)out + oo) = ov;
    }
    STAMP(1, 7);
}

__global__ __launch_bounds__(512, 4) void fused_fwd(const u16* __restrict__ x, const u16* __restrict__ wqkv, const u16* __restrict__ wo,
                                                     const u16* __restrict__ wg, const u16* __restrict__ wu, const u16* __restrict__ wd,
                                                     const u16* __restrict__ gw, char* __restrict__ ws, u16* __restrict__ out) {
    extern __shared__ __attribute__((aligned(16))) char lds[];
    const char* code;
    asm volatile("s_getpc_b64 %0" : "=s"(code));
    if (blockIdx.x < 256) role_a(blockIdx.x, x, wqkv, gw, wd, wo, ws, lds, code);
    else role_f(blockIdx.x - 256, x, wqkv, wo, wg, wu, wd, ws, out, lds);
}

extern "C" void kernel_launch(void* const* d_in, const int* in_sizes, int n_in, void* d_out, int out_size, void* d_ws, size_t ws_size,
                              hipStream_t stream) {
    (void)hipFuncSetAttribute((const void*)fused_fwd, hipFuncAttributeMaxDynamicSharedMemorySize, LDS_BYTES);
    fused_fwd<<<512, 512, LDS_BYTES, stream>>>((const u16*)d_in[0], (const u16*)d_in[1], (const u16*)d_in[2], (const u16*)d_in[4],
                                               (const u16*)d_in[5], (const u16*)d_in[6], (const u16*)d_in[7], (char*)d_ws, (u16*)d_out);
}
```

```cpp
#include <hip/hip_runtime.h>
#include <hip/hip_bf16.h>
#include <stdint.h>

typedef short bf16x8 __attribute__((ext_vector_type(8)));
typedef short s16x4 __attribute__((ext_vector_type(4)));
typedef float f32x16 __attribute__((ext_vector_type(16)));
typedef float f32x4 __attribute__((ext_vector_type(4)));
typedef float f32x2 __attribute__((ext_vector_type(2)));
typedef unsigned u32x4 __attribute__((ext_vector_type(4)));
typedef unsigned u32x2 __attribute__((ext_vector_type(2)));
typedef __bf16 bf16x2_t __attribute__((ext_vector_type(2)));
typedef unsigned short u16;
typedef unsigned long long u64;
#define LAS __attribute__((address_space(3)))
#define GAS __attribute__((address_space(1)))
#define DI __device__ __forceinline__
#define MFMA32(a, b, c) __builtin_amdgcn_mfma_f32_32x32x16_bf16((a), (b), (c), 0, 0, 0)
#define MFMA16(a, b, c) __builtin_amdgcn_mfma_f32_16x16x32_bf16((a), (b), (c), 0, 0, 0)
#define SBAR() __builtin_amdgcn_sched_barrier(0)
#define PIN(x) asm volatile("" : "+v"(x))
#define BC8(x) __builtin_bit_cast(bf16x8, x)
template <int V> struct IC { static constexpr int value = V; };
template <int I, int N, class F> DI void static_for(F&& f) { if constexpr (I < N) { f(IC<I>{}); static_for<I + 1, N>(f); } }

constexpr int SEQ = 2048, DM = 512, FF = 1792;
constexpr unsigned WS_E = 0;
constexpr unsigned WS_KVF = 4096;
constexpr unsigned WS_HF = WS_KVF + 8192;
constexpr unsigned WS_PF = WS_HF + 16384;
constexpr unsigned WS_G = WS_PF + 4096;
constexpr unsigned WS_KV = 65536;
constexpr unsigned WS_H = WS_KV + (4u << 20);
constexpr unsigned WS_O = WS_H + 128u * 56 * 1024;
constexpr unsigned WS_END = WS_O + 128u * 16 * 1024;
constexpr float QSCALE = 0.25f * 1.4426950408889634f;
constexpr int LDS_BYTES = 81920;
constexpr unsigned EMPTY = 0xffffffffu;

DI unsigned cvtpk(float lo, float hi) { const f32x2 v = {lo, hi}; return __builtin_bit_cast(unsigned, __builtin_convertvector(v, bf16x2_t)); }
DI s16x4 tr_read(LAS const char* p) { return __builtin_amdgcn_ds_read_tr16_b64_v4i16((LAS s16x4*)p); }
DI bf16x8 cat8(s16x4 a, s16x4 b) { return __builtin_shufflevector(a, b, 0, 1, 2, 3, 4, 5, 6, 7); }
DI void dma16(unsigned voff, const void* sbase, unsigned lds) {
    asm volatile("s_mov_b32 m0, %2\n\ts_nop 0\n\tglobal_load_lds_dwordx4 %0, %1" :: "v"(voff), "s"(sbase), "s"(lds) : "memory");
}
DI void dma16_dev(unsigned voff, const void* sbase, unsigned lds) {
    asm volatile("s_mov_b32 m0, %2\n\ts_nop 0\n\tglobal_load_lds_dwordx4 %0, %1 sc1" :: "v"(voff), "s"(sbase), "s"(lds) : "memory");
}
DI void dma64_dev(unsigned voff, const void* sbase, unsigned lds) {
    asm volatile("s_mov_b32 m0, %2\n\ts_nop 0\n\tglobal_load_lds_dwordx4 %0, %1 sc1\n\tglobal_load_lds_dwordx4 %0, %1 offset:1024 sc1\n\t"
                 "global_load_lds_dwordx4 %0, %1 offset:2048 sc1\n\tglobal_load_lds_dwordx4 %0, %1 offset:3072 sc1" :: "v"(voff), "s"(sbase), "s"(lds) : "memory");
}
DI void dma16_48(unsigned voff, const void* sbase, unsigned lds) {
    asm volatile("s_mov_b32 m0, %2\n\ts_mov_b32 exec_hi, 0xffff\n\tglobal_load_lds_dwordx4 %0, %1\n\ts_mov_b32 exec_hi, -1" :: "v"(voff), "s"(sbase), "s"(lds) : "memory");
}
DI void touch(unsigned voff, const void* sbase, unsigned lds) {
    asm volatile("s_mov_b32 m0, %2\n\ts_nop 0\n\tglobal_load_lds_dword %0, %1" :: "v"(voff), "s"(sbase), "s"(lds) : "memory");
}
template <int N> DI void wait_vm_bar() { asm volatile("s_waitcnt vmcnt(%0) lgkmcnt(0)\n\ts_barrier" :: "n"(N) : "memory"); }
#define WAIT_VM(N) asm volatile("s_waitcnt vmcnt(" #N ")" ::: "memory")
#define WAIT_VM_BAR(N) asm volatile("s_waitcnt vmcnt(" #N ") lgkmcnt(0)\n\ts_barrier" ::: "memory")
#define LGKM_BAR() asm volatile("s_waitcnt lgkmcnt(0)\n\ts_barrier" ::: "memory")
#define BAR() asm volatile("s_barrier" ::: "memory")
typedef __amdgpu_buffer_rsrc_t rsrc_t;
DI void st16_dev(rsrc_t r, unsigned off, u32x4 v) { __builtin_amdgcn_raw_buffer_store_b128(v, r, off, 0, 16); }
DI u32x4 ld16_dev(rsrc_t r, unsigned voff, unsigned soff) { return __builtin_amdgcn_raw_buffer_load_b128(r, voff, soff, 16); }
DI void flag_set(char* ws, unsigned off, u64 tag) { __hip_atomic_store((GAS u64*)(ws + off), tag, __ATOMIC_RELAXED, __HIP_MEMORY_SCOPE_AGENT); }
DI u64 flag_get(const char* ws, unsigned off) {
    u64 v;
    asm volatile("global_atomic_or_x2 %0, %1, %3, %2 sc0 sc1\n\ts_waitcnt vmcnt(0)" : "=&v"(v) : "v"(off), "s"(ws), "v"(0ull) : "memory");
    return v;
}
DI u64 flag_peek(const char* ws, unsigned off) { return __hip_atomic_load((const GAS u64*)(ws + off), __ATOMIC_RELAXED, __HIP_MEMORY_SCOPE_AGENT); }
template <int NAP, int LONG_NAP = NAP> DI void flag_wait(const char* ws, unsigned off, u64 tag) {
    const u64 t0 = __builtin_amdgcn_s_memrealtime();
    for (;;) {
        const u64 got = __ballot(flag_peek(ws, off) == tag);
        if (got == ~0ull) break;
        if (__builtin_amdgcn_s_memrealtime() - t0 > 2000000ull) break;
        if (LONG_NAP != NAP && got == 0) __builtin_amdgcn_s_sleep(LONG_NAP); else __builtin_amdgcn_s_sleep(NAP);
    }
    asm volatile("" ::: "memory");
}
template <int NAP> DI u64 flag_wait_some(const char* ws, unsigned off, u64 tag, u64 want) {
    const u64 t0 = __builtin_amdgcn_s_memrealtime();
    u64 got;
    for (;;) {
        got = __ballot(flag_get(ws, off) == tag);
        if ((got & want) == want || __builtin_amdgcn_s_memrealtime() - t0 > 2000000ull) break;
        __builtin_amdgcn_s_sleep(NAP);
    }
    asm volatile("" ::: "memory");
    return got;
}
DI u64 read_tag(const char* ws) {
    u64 e;
    asm volatile("s_load_dwordx2 %0, %1, 0x0\n\ts_waitcnt lgkmcnt(0)" : "=s"(e) : "s"(ws) : "memory");
    return e * 6364136223846793005ull + 1442695040888963407ull;
}

DI int fresh_lane() { int l; asm volatile("v_mbcnt_lo_u32_b32 %0, -1, 0\n\tv_mbcnt_hi_u32_b32 %0, -1, %0" : "=v"(l)); return l; }
#define LANE16() const int lane = fresh_lane(), r16 = lane & 15, g4 = lane >> 4, q4 = r16 >> 2, p4 = lane & 3; (void)r16; (void)g4; (void)q4; (void)p4

#define STAMP(k, i) do { } while (0)
#define STAMP3(k, i) do { } while (0)
#define LSTAMP(j) do { } while (0)
#define LDECL() do { } while (0)
#define LFLUSH(k) do { } while (0)

#define KV_POLLER 0
#define MOVER 1
#define KV_FIRST 16
#define KV_NAP 3
constexpr int P_W = 49152;
constexpr int P_GW = P_W + 5 * 6144;
constexpr int A_SLOT = 17408;
constexpr int A_Q = 4 * A_SLOT;

DI f32x16 attn_exact(rsrc_t rs, unsigned kvbase, bf16x8 qf, int lane) {
    const int r32 = lane & 31, hh = lane >> 5;
    f32x16 o, zero;
#pragma unroll
    for (int i = 0; i < 16; ++i) { o[i] = 0.f; zero[i] = 0.f; }
    float m = -INFINITY;
    const unsigned cst = ((r32 & 27) == 16) ? 0x3f803f80u : 0u;
    const u32x4 cv = {cst, cst, cst, cst};
#pragma unroll 1
    for (int t = 0; t < 64; ++t) {
        const unsigned tb = kvbase + (t >> 2) * 16384 + ((t >> 1) & 1) * 4096;
        const bf16x8 kf = BC8(ld16_dev(rs, tb + (t & 1) * 1024 + lane * 16, 0));
        const f32x16 s = MFMA32(kf, qf, zero);
        float tm = s[0];
#pragma unroll
        for (int i = 1; i < 16; ++i) tm = fmaxf(tm, s[i]);
        tm = fmaxf(tm, __shfl_xor(tm, 32));
        const float mn = fmaxf(m, tm);
        const float alpha = __builtin_amdgcn_exp2f(m - mn);
        m = mn;
#pragma unroll
        for (int i = 0; i < 16; ++i) o[i] *= alpha;
#pragma unroll
        for (int st = 0; st < 2; ++st) {
            u32x4 pw;
#pragma unroll
            for (int j = 0; j < 4; ++j) pw[j] = cvtpk(__builtin_amdgcn_exp2f(s[8 * st + 2 * j] - m), __builtin_amdgcn_exp2f(s[8 * st + 2 * j + 1] - m));
            const u32x4 vv = ld16_dev(rs, tb + 8192 + hh * 1024 + ((t & 1) * 2 + st) * 256 + (r32 & 15) * 16, 0);
            o = MFMA32(BC8(r32 < 16 ? vv : cv), BC8(pw), o);
        }
    }
    return o;
}

#define CODE_KB 44
#define PO_Q 1
DI void role_a(int blk, const u16* __restrict__ x, const u16* __restrict__ wqkv, const u16* __restrict__ gw,
               const u16* __restrict__ wd, const u16* __restrict__ wo, char* __restrict__ ws, char* lds, const char* code) {
    const int lane = fresh_lane(), wid = __builtin_amdgcn_readfirstlane(threadIdx.x >> 6);
    const int r32 = lane & 31, hh = lane >> 5;
    const int xcd = blk & 7, bi = blk >> 3;
    const int qb = (xcd & 1) * 8 + (bi & 7), hp = (xcd >> 1) * 4 + (bi >> 3);
    const unsigned lds0 = (unsigned)(uintptr_t)lds;
    LAS char* L = (LAS char*)lds;
    const rsrc_t rs = __builtin_amdgcn_make_buffer_rsrc(ws, 0, WS_END, 0x00020000);
    const unsigned kvb = WS_KV + hp * 262144;
    const unsigned kvown = kvb + qb * 16384;
    u64 tag;
    STAMP(0, 0);
    __builtin_amdgcn_s_setprio(3);
#define LD8(A) (*(LAS const bf16x8*)(L + (A)))
    {
        const int lw = 2 * (wid & 3);
        const int xrow = 8 * lw + (lane >> 3);
        const unsigned xo = (unsigned)(xrow * 1024 + (((lane & 7) ^ ((xrow >> 1) & 7)) << 4)), xo2 = (xo ^ 64) + 8192;
        const unsigned wo_ = (unsigned)((4 * lw + ((lane >> 2) & 3)) * 3072 + (lane >> 4) * 1024 + (lane & 3) * 16);
        const char* xb = (const char*)x + (size_t)qb * 128 * 1024;
        const char* wb = (const char*)wqkv + hp * 64;
        auto ld_x = [&](int h) {
            const char* b = xb + (h & 1) * 65536 + (h >> 1) * 128;
            const unsigned d = lds0 + (((h >> 1) + 1) % 3) * 16384 + (h & 1) * 8192 + lw * 1024;
            dma16(xo, b, d); dma16(xo2, b, d + 1024);
        };
        auto ld_w = [&](int j) {
            const char* b = wb + (size_t)j * (32 * 3072);
            const unsigned d = lds0 + P_W + (j % 5) * 6144 + lw * 768;
            dma16_48(wo_, b, d); dma16_48(wo_, b + 4 * 3072, d + 768);
        };
        if (wid >= 4) {
#pragma unroll
            for (int j = 0; j < 5; ++j) { ld_x(j); ld_w(j); }
            ld_x(5);
        } else {
            if (wid == 3) dma16((unsigned)(lane * 16), gw, lds0 + P_GW);
            if (bi * 4 + wid < CODE_KB / 2) touch((unsigned)(((bi * 4 + wid) * 16 + (lane & 15)) * 128), code, lds0 + P_GW + 1024 + wid * 256);
        }
        tag = read_tag(ws);
        auto proj = [&](auto KIND) {
            constexpr int kind = decltype(KIND)::value, NT = kind == 0 ? 1 : 2;
            const int row0 = kind == 0 ? (wid - 4) * 32 : (wid & 1) * 64;
            int xa[4];
#pragma unroll
            for (int k = 0; k < 4; ++k) { xa[k] = ((row0 + r32) * 128 + ((hh ^ ((r32 >> 1) & 7)) << 4)) ^ (k << 5); PIN(xa[k]); }
            const int wa = P_W + hh * 1536 + kind * 256 + ((lane & 15) >> 2) * 64 + ((lane >> 4) & 1) * 32 + (lane & 3) * 8;
            int ga = (8 * hp + (lane >> 3)) * 128 + (((lane & 7) ^ ((4 * hp + (lane >> 4)) & 7)) << 4), gb = P_GW + (lane & 7) * 16;
            if (kind == 2) { PIN(ga); PIN(gb); }
            f32x16 acc[NT];
#pragma unroll
            for (int t = 0; t < NT; ++t)
#pragma unroll
                for (int i = 0; i < 16; ++i) acc[t][i] = 0.f;
            bf16x8 xf[2][2][NT], wf[2][2];
            u32x4 gx, gv;
            float z = 0.f;
            LDECL();
            static_for<0, 17>([&](auto J) {
                constexpr int j = decltype(J)::value, c = j & 1;
                constexpr int xs = (((j >> 1) + 1) % 3) * 16384, wsl = (j % 5) * 6144;
                if constexpr (j < 16) {
                    constexpr int issued = j < 2 ? 11 : (2 * j + 8 < 32 ? 2 * j + 8 : 32);
                    wait_vm_bar<kind == 0 ? 2 * (issued - 1 - (c ? 2 * j + 1 : 2 * j + 2)) : 0>();
                    LSTAMP(j);
                }
                auto mm = [&](int ks, int t) {
                    if constexpr (j > 0) acc[t] = kind == 2 ? MFMA32(xf[c ^ 1][ks][t], wf[c ^ 1][ks], acc[t]) : MFMA32(wf[c ^ 1][ks], xf[c ^ 1][ks][t], acc[t]);
                    SBAR();
                };
                auto rw = [&](int ks) { if constexpr (j < 16) wf[c][ks] = cat8(tr_read(L + wa + wsl + ks * 3072), tr_read(L + wa + wsl + ks * 3072 + 768)); };
                auto rx = [&](int ks, int t) { if constexpr (j < 16) xf[c][ks][t] = LD8(xa[2 * c + ks] + xs + t * 4096); };
                if constexpr (kind == 0) {
                    rw(0); rx(0, 0); rw(1); rx(1, 0);
                    SBAR();
                    if constexpr (j + 4 < 16 && j >= 2) ld_x(j + 4);
                    mm(0, 0);
                    if constexpr (j + 4 < 16 && j >= 1) ld_w(j + 4);
                    mm(1, 0);
                } else {
                    mm(0, 0); rw(0); rx(0, 0); SBAR();
                    mm(0, 1); rx(0, 1); rw(1); SBAR();
                    mm(1, 0); rx(1, 0); rx(1, 1);
                    if (kind == 2 && j < 16 && c == 0 && wid == 3) { gx = *(LAS const u32x4*)(L + ga + xs); gv = *(LAS const u32x4*)(L + gb + (j >> 1) * 128); }
                    SBAR();
                    mm(1, 1);
                    if (kind == 2 && c == 1 && wid == 3) {
#pragma unroll
                        for (int e = 0; e < 4; ++e) {
                            z = fmaf(__uint_as_float(gx[e] << 16), __uint_as_float(gv[e] << 16), z);
                            z = fmaf(__uint_as_float(gx[e] & 0xffff0000u), __uint_as_float(gv[e] & 0xffff0000u), z);
                        }
                    }
                }
            });
#pragma unroll
            for (int i = 0; i < NT; ++i)
#pragma unroll
                for (int e = 0; e < 2; ++e) {
                    u32x4 v;
#pragma unroll
                    for (int j = 0; j < 4; ++j)
                        v[j] = kind == 0 ? cvtpk(acc[i][8 * e + 2 * j] * QSCALE, acc[i][8 * e + 2 * j + 1] * QSCALE) : cvtpk(acc[i][8 * e + 2 * j], acc[i][8 * e + 2 * j + 1]);
                    if constexpr (kind == 0) *(LAS u32x4*)(L + A_Q + ((wid - 4) * 2 + e) * 1024 + lane * 16) = v;
                    else {
                        const int tg = wid & 1;
                        const int off = kind == 1 ? tg * 4096 + e * 2048 + i * 1024 + lane * 16
                                                  : 8192 + tg * 4096 + (r32 >> 4) * 2048 + hh * 1024 + (2 * i + e) * 256 + (r32 & 15) * 16;
                        st16_dev(rs, kvown + off, v);
                        *(LAS u32x4*)(L + off) = v;
                    }
                }
            if (kind == 1) LFLUSH(3);
            if (kind == 2 && wid == 3) {
                z += __shfl_xor(z, 1); z += __shfl_xor(z, 2); z += __shfl_xor(z, 4);
                if ((lane & 7) == 0) __builtin_amdgcn_raw_buffer_store_b32(__float_as_uint(1.f / (1.f + __expf(-z))), rs, WS_G + (unsigned)(qb * 128 + 8 * hp + (lane >> 3)) * 4, 0, 16);
            }
        };
        if (wid >= 4) proj(IC<0>{}); else if (wid < 2) proj(IC<1>{}); else proj(IC<2>{});
    }
    const unsigned cst = ((lane & 11) == 0) ? 0x3f803f80u : 0u;
    if (wid == 4) *(LAS u32x4*)(L + 16384 + lane * 16) = (u32x4){cst, cst, cst, cst};
    LGKM_BAR();
    STAMP(0, 1);
    const bool mover = (wid >> 2) == MOVER;
    const char* kvg = ws + kvb + (wid & 3) * 4096;
    const unsigned doff = (unsigned)(lane * 16);
    auto fetch = [&](int c, int slot) {
        dma64_dev(doff, kvg + (size_t)((qb + c) & 15) * 16384, lds0 + slot * A_SLOT + (wid & 3) * 4096);
    };
    if (wid > 4) *(LAS u32x4*)(L + (wid - 4) * A_SLOT + 16384 + lane * 16) = (u32x4){cst, cst, cst, cst};
    const int hsel = wid & 1, qp = (wid >> 1) & 1, kh = wid >> 2;
    const bf16x8 qA = *(LAS const bf16x8*)(L + A_Q + ((2 * qp) * 2 + hsel) * 1024 + lane * 16);
    const bf16x8 qB = *(LAS const bf16x8*)(L + A_Q + ((2 * qp + 1) * 2 + hsel) * 1024 + lane * 16);
    int ka = kh * 4096 + hsel * 2048 + lane * 16;
    int va = r32 < 16 ? 8192 + kh * 4096 + hsel * 2048 + hh * 1024 + r32 * 16 : 16384 + (r32 & 15) * 16;
    PIN(ka); PIN(va);
    bf16x8 kf = LD8(ka);
    f32x16 negm, oA, oB, sX, sY;
    float mref;
    {
        f32x16 zero;
#pragma unroll
        for (int i = 0; i < 16; ++i) { zero[i] = 0.f; oA[i] = 0.f; oB[i] = 0.f; }
        sX = MFMA32(kf, qA, zero); sY = MFMA32(kf, qB, zero);
        float tm = fmaxf(sX[0], sY[0]);
#pragma unroll
        for (int i = 1; i < 16; ++i) tm = fmaxf(tm, fmaxf(sX[i], sY[i]));
        mref = fmaxf(tm, __shfl_xor(tm, 32));
#pragma unroll
        for (int i = 0; i < 16; ++i) negm[i] = -mref;
        PIN(negm);
    }
    sX = MFMA32(kf, qA, negm);
    bf16x8 v0 = LD8(va), v1 = v0;
    u32x4 p0, p1 = {0u, 0u, 0u, 0u};
    auto publish = [&]() {
        WAIT_VM(0);
        if (fresh_lane() == 0) flag_set(ws, WS_KVF + hp * 512 + (unsigned)(wid * 16 + qb) * 8, tag);
    };
    if (wid < 4) publish();
    __builtin_amdgcn_s_setprio(0);
    auto o_slot = [&](int l) { return WS_O + (unsigned)((8 * qb + 4 * qp + 2 * kh + ((l >> 4) & 1)) * 16 + hp) * 1024 + ((2 * hsel + (l >> 5)) * 16 + (l & 15)) * 16; };
#define EX2(X, i) X[i] = __builtin_amdgcn_exp2f(X[i])
#define HALF(SC, M1, L1, M2, L2, M3, L3) do { \
        EX2(SC, 0); EX2(SC, 1); PIN(SC); SBAR(); \
        M1; SBAR(); L1; \
        EX2(SC, 2); EX2(SC, 3); EX2(SC, 4); EX2(SC, 5); EX2(SC, 6); EX2(SC, 7); PIN(SC); SBAR(); \
        M2; SBAR(); L2; \
        p0[0] = cvtpk(SC[0], SC[1]); p0[1] = cvtpk(SC[2], SC[3]); p0[2] = cvtpk(SC[4], SC[5]); p0[3] = cvtpk(SC[6], SC[7]); PIN(p0); SBAR(); \
        EX2(SC, 8); EX2(SC, 9); EX2(SC, 10); EX2(SC, 11); PIN(SC); SBAR(); \
        M3; SBAR(); L3; \
        EX2(SC, 12); EX2(SC, 13); EX2(SC, 14); EX2(SC, 15); \
        p1[0] = cvtpk(SC[8], SC[9]); p1[1] = cvtpk(SC[10], SC[11]); p1[2] = cvtpk(SC[12], SC[13]); p1[3] = cvtpk(SC[14], SC[15]); PIN(p1); SBAR(); \
    } while (0)
#define CHUNK(S0, S1, SYNC) do { \
        HALF(sX, oB = MFMA32(v1, BC8(p1), oB), v1 = LD8(va + (S0) + 256), sY = MFMA32(kf, qB, negm), kf = LD8(ka + (S0) + 1024), oA = MFMA32(v0, BC8(p0), oA), (void)0); \
        HALF(sY, oA = MFMA32(v1, BC8(p1), oA), (void)0, sX = MFMA32(kf, qA, negm), (void)0, oB = MFMA32(v0, BC8(p0), oB), v0 = LD8(va + (S0) + 512)); \
        HALF(sX, oB = MFMA32(v1, BC8(p1), oB), v1 = LD8(va + (S0) + 768), sY = MFMA32(kf, qB, negm), SYNC; kf = LD8(ka + (S1)), oA = MFMA32(v0, BC8(p0), oA), (void)0); \
        HALF(sY, oA = MFMA32(v1, BC8(p1), oA), (void)0, sX = MFMA32(kf, qA, negm), (void)0, oB = MFMA32(v0, BC8(p0), oB), v0 = LD8(va + (S1))); \
    } while (0)
    auto kv_wait = [&](int c, int cnt) {
        const unsigned m = ((1u << cnt) - 1) << ((qb + c) & 15);
        flag_wait_some<KV_NAP>(ws, WS_KVF + hp * 512 + (unsigned)fresh_lane() * 8, tag,
            (u64)((m | m >> 16) & 0xffffu) * 0x0001000100010001ull);
    };
    auto warm = [&](int q) {
        const int idx = bi * 2 + (wid & 3);
        if ((wid & 3) < 2 && q >= 1 && q <= 5 && (q == 5 || idx < 56))
            touch(doff * 8, (q == 5 ? (const char*)wo : (const char*)wd + (q - 1) * 458752) + idx * 8192, lds0 + A_Q + wid * 256);
    };
    auto late = [&](int q) {
        if (q == PO_Q) {
#pragma unroll
            for (int e = 0; e < 2; ++e)
                __builtin_amdgcn_raw_buffer_store_b128((u32x4){EMPTY, EMPTY, EMPTY, EMPTY}, rs, doff, WS_O + (unsigned)((8 * qb + 2 * (wid & 3) + e) * 16 + hp) * 1024, 16);
        }
        if (q == PO_Q + 1 && (wid & 3) == 3 && fresh_lane() == 0) flag_set(ws, WS_PF + qb * 128 + hp * 8, tag);
    };
#pragma unroll
    for (int n = 0; n < 4; ++n) {
        auto sync_a = [&]() {
            if (n == 0) {
                STAMP(0, 2);
                if (wid == KV_POLLER) kv_wait(1, KV_FIRST);
                BAR();
                STAMP(0, 3);
                fetch(1 + (wid >> 2), 1 + (wid >> 2));
            }
            STAMP3(2, 4 * n);
            WAIT_VM_BAR(0);
            STAMP3(2, 4 * n + 1);
            if (n == 0) STAMP(0, 4);
            if (mover) { fetch(4 * n + 3, 3); if (n < 3) fetch(4 * n + 4, 0); }
            else { warm(2 * n); late(2 * n); }
        };
        auto sync_b = [&]() {
            if (KV_FIRST < 16 && n == 0 && wid == KV_POLLER) kv_wait(0, 16);
            STAMP3(2, 4 * n + 2);
            WAIT_VM_BAR(0);
            STAMP3(2, 4 * n + 3);
            if (mover) { if (n < 3) { fetch(4 * n + 5, 1); fetch(4 * n + 6, 2); } }
            else { warm(2 * n + 1); late(2 * n + 1); }
        };
        CHUNK(0, A_SLOT, sync_a());
        CHUNK(A_SLOT, 2 * A_SLOT, (void)0);
        CHUNK(2 * A_SLOT, 3 * A_SLOT, sync_b());
        CHUNK(3 * A_SLOT, 0, (void)0);
    }
    oB = MFMA32(v1, BC8(p1), oB);
    STAMP(0, 5);
    {
        LAS float* ex = (LAS float*)(L + A_SLOT + wid * 2560) + lane;
#pragma unroll
        for (int i = 0; i < 9; ++i) ex[i * 64] = kh ? oA[i] : oB[i];
        ex[9 * 64] = mref;
    }
    LGKM_BAR();
    f32x16 o;
    {
        LAS const float* ex = (LAS const float*)(L + A_SLOT + (wid ^ 4) * 2560) + lane;
        const float mo = ex[9 * 64], mm = fmaxf(mref, mo);
        const float f1 = __builtin_amdgcn_exp2f(mref - mm), f2 = __builtin_amdgcn_exp2f(mo - mm);
#pragma unroll
        for (int i = 0; i < 9; ++i) o[i] = (kh ? oB[i] : oA[i]) * f1 + ex[i * 64] * f2;
        bool ok = (kh ? oB[8] : oA[8]) < 1.0995116e12f && ex[8 * 64] < 1.0995116e12f && o[8] > 9.0949470e-13f;
#pragma unroll
        for (int i = 0; i < 8; ++i) ok = ok && (fabsf(o[i]) < 3.0e38f);
        if (__builtin_expect(!__all(ok), 0)) o = attn_exact(rs, kvb + hsel * 2048, kh ? qB : qA, lane);
    }
    {
        const float inv = 1.f / o[8];
        u32x4 w;
#pragma unroll
        for (int j = 0; j < 4; ++j) w[j] = min(cvtpk(o[2 * j] * inv, o[2 * j + 1] * inv), EMPTY - 1);
        st16_dev(rs, o_slot(lane), w);
    }
    STAMP(0, 6);
    if (blk == 0 && wid == 7) {
        const u64 te = __builtin_amdgcn_s_memrealtime();
        for (;;) {
            bool ok = true;
#pragma unroll
            for (int i = 0; i < 4; ++i) {
                const int w = i * 64 + lane;
                ok = ok & (flag_peek(ws, WS_KVF + (w >> 4) * 512 + (w & 15) * 8) == tag) & (flag_peek(ws, WS_HF + (w >> 4) * 1024 + (w & 15) * 32) == tag);
            }
            if (__all(ok) || __builtin_amdgcn_s_memrealtime() - te > 2000000ull) break;
            __builtin_amdgcn_s_sleep(8);
        }
        if (lane == 0) flag_set(ws, WS_E, tag);
    }
}

#define F_DELAYA 4
#define F_PRIO_DN 0
#define O_NAP 4
#define WFD 3
#define O_PRIO 0
#define F_T0 30
#define F_T1 500
#define F_HS 0
#define F_PRIO 2
#define F_HI() __builtin_amdgcn_s_setprio(F_PRIO)
#define F_LO() __builtin_amdgcn_s_setprio(0)
DI void role_f(int blk, const u16* __restrict__ x, const u16* __restrict__ wqkv, const u16* __restrict__ wo, const u16* __restrict__ wg, const u16* __restrict__ wu,
               const u16* __restrict__ wd, char* __restrict__ ws, u16* __restrict__ out, char* lds) {
    const int wid = __builtin_amdgcn_readfirstlane(threadIdx.x >> 6);
    const int xcd = blk & 7, bi = blk >> 3;
    const int cq = xcd >> 1, rb = (xcd & 1) * 8 + (bi & 7), cb = cq * 4 + (bi >> 3);
    const int mb = 4 * xcd + (bi >> 3), nb = bi & 7;
    const unsigned lds0 = (unsigned)(uintptr_t)lds;
    LAS char* L = (LAS char*)lds;
    const rsrc_t rs = __builtin_amdgcn_make_buffer_rsrc(ws, 0, WS_END, 0x00020000);
    STAMP(1, 0);
    const u64 tstart = __builtin_amdgcn_s_memrealtime();
    auto wait_until = [&](unsigned ticks) { while ((unsigned)(__builtin_amdgcn_s_memrealtime() - tstart) < ticks) __builtin_amdgcn_s_sleep(2); };
    F_HI();
    {
        const int lane = fresh_lane(), idx = bi * 8 + wid;
        __builtin_amdgcn_s_sleep(F_DELAYA);
        const int xl = lane < 20 ? lane : 19;
        touch((unsigned)(idx * 4096 + (xl & 3) * 1024 + (3 + (xl >> 2)) * 128), (const char*)x + (size_t)(xcd & 1) * (1 << 20), lds0 + wid * 1024);
        const int q = lane < 12 ? lane : 11, qr = q >= 6, qc = q - 6 * qr;
        if (idx >= 80) touch((unsigned)((2 * idx + qr) * 3072 + (qc >> 1) * 1024 + (qc & 1) * 128), (const char*)wqkv + cq * 256, lds0 + wid * 1024);
    }
    const u64 tag = read_tag(ws);
    {
        LANE16();
        const int r32 = lane & 31, hh = lane >> 5;
        const int xrow = 8 * wid + (lane >> 3);
        unsigned xo = (unsigned)(xrow * 1024 + (((lane & 7) ^ ((xrow >> 1) & 7)) << 4));
        const int bg = (r16 >> 1) ^ (2 * g4), bu = bg ^ 1;
        const unsigned wrow = (unsigned)((4 * wid + g4) * (FF * 2) + (lane & 1) * 16);
        const int odd = cb & 1;
        unsigned go = wrow + bg * 32, uo = wrow + bu * 32;
        PIN(xo); PIN(go); PIN(uo);
        const char* xb = (const char*)x + (size_t)rb * 128 * 1024;
        const char* gb = (const char*)wg + cb * 224 - odd * 32;
        const char* ub = (const char*)wu + cb * 224 - odd * 32;
        const unsigned dw = lds0 + wid * 1024;
        auto ld_x = [&](int s) { const unsigned d = dw + (s & 1) * 16384; dma16(xo, xb + s * 128, d); dma16(xo, xb + 65536 + s * 128, d + 8192); };
        auto ld_w = [&](int j) {
            const unsigned d = dw + 32768 + (j % 3) * 16384; const size_t o = (size_t)j * (32 * FF * 2); dma16(go, gb + o, d); dma16(uo, ub + o, d + 8192); };
        auto gu = [&](auto KIND) {
            constexpr int NT = decltype(KIND)::value, NU = 4 - NT;
            const int t0 = NT == 2 ? (wid & 1) * 2 : wid - 4, u0 = NT == 2 ? (wid >> 1) * 2 : 4;
            int xa[4], wa[NU];
#pragma unroll
            for (int k = 0; k < 4; ++k) { xa[k] = ((t0 * 32 + r32) * 128 + ((hh ^ ((r32 >> 1) & 7)) << 4)) ^ (k << 5); PIN(xa[k]); }
#pragma unroll
            for (int u = 0; u < NU; ++u) { wa[u] = 32768 + (g4 & 1) * 8192 + (8 * hh + q4) * 256 + (((u0 + u + odd) ^ (2 * q4 + (g4 & 1))) << 5) + p4 * 8; PIN(wa[u]); }
            f32x16 acc[NT][NU];
#pragma unroll
            for (int t = 0; t < NT; ++t)
#pragma unroll
                for (int u = 0; u < NU; ++u)
#pragma unroll
                    for (int i = 0; i < 16; ++i) acc[t][u][i] = 0.f;
            bf16x8 x0[NT], w0[NU], x1[2][NT], w1[2][NU];
            auto rd = [&](auto J, int ks, bf16x8 (&xf)[NT], bf16x8 (&wf)[NU]) {
                constexpr int j = decltype(J)::value;
#pragma unroll
                for (int t = 0; t < NT; ++t) xf[t] = LD8(xa[2 * (j & 1) + ks] + ((j >> 1) & 1) * 16384 + t * 4096);
#pragma unroll
                for (int u = 0; u < NU; ++u) { const int a = wa[u] + (j % 3) * 16384 + ks * 4096; wf[u] = cat8(tr_read(L + a), tr_read(L + a + 1024)); }
            };
            auto mm = [&](bf16x8 (&xf)[NT], bf16x8 (&wf)[NU]) {
#pragma unroll
                for (int t = 0; t < NT; ++t)
#pragma unroll
                    for (int u = 0; u < NU; ++u) acc[t][u] = MFMA32(wf[u], xf[t], acc[t][u]);
            };
            {
                const int g = lane < 14 ? lane : 13, gr = g >= 7, gc = g - 7 * gr;
                unsigned po = (unsigned)((2 * (bi * 8 + wid) + gr) * (FF * 2) + gc * 128);
                PIN(po);
                wait_until(F_T0);
                touch(po, (const char*)wg + cq * 896, dw);
                touch(po, (const char*)wu + cq * 896, dw);
                wait_until(F_T1);
            }
            ld_x(0); ld_w(0);
            BAR();
            ld_w(1); ld_x(1); ld_w(2);
            if (F_HS == 0) F_LO();
            LDECL();
            WAIT_VM_BAR(6);
            STAMP(1, 1);
            rd(IC<0>{}, 0, x0, w0); rd(IC<0>{}, 1, x1[0], w1[0]);
            static_for<0, 16>([&](auto J) {
                constexpr int j = decltype(J)::value, c = j & 1;
                wait_vm_bar<j >= 14 ? 0 : (c ? 2 : 4)>();
                LSTAMP(j);
                if constexpr (F_HS > 0 && j == F_HS) F_LO();
                if constexpr (c == 0) { if constexpr (j + 3 < 16) ld_w(j + 3); }
                else if constexpr (j + 3 < 16) { ld_x((j + 3) >> 1); ld_w(j + 3); }
                if constexpr (j < 15) rd(IC<j + 1>{}, 1, x1[c ^ 1], w1[c ^ 1]);
                SBAR();
                mm(x0, w0);
                SBAR();
                if constexpr (j < 15) rd(IC<j + 1>{}, 0, x0, w0);
                SBAR();
                mm(x1[c], w1[c]);
                SBAR();
            });
            F_HI();
            STAMP(1, 2);
            const int lane = fresh_lane();
#pragma unroll
            for (int t = 0; t < NT; ++t)
#pragma unroll
                for (int u = 0; u < NU; ++u) {
                    u32x4 v;
#pragma unroll
                    for (int j = 0; j < 4; ++j) v[j] = cvtpk(acc[t][u][2 * j] * acc[t][u][8 + 2 * j], acc[t][u][2 * j + 1] * acc[t][u][9 + 2 * j]);
                    st16_dev(rs, WS_H + (unsigned)((rb * 4 + t0 + t) * 112 + 7 * cb + u0 + u) * 1024 + lane * 16, v);
                }
            WAIT_VM(0);
            if (NT == 2) LFLUSH(2);
            if (lane == 0) flag_set(ws, WS_HF + (2 * rb + (t0 >> 1)) * 512 + (unsigned)(cb * 4 + (NT == 2 ? wid >> 1 : 2 + (wid & 1))) * 8, tag);
        };
        if (wid < 4) gu(IC<2>{}); else gu(IC<1>{});
    }
#define WDO() (unsigned)((lane >> 3) * 1024 + ((((lane & 7) >> 1) ^ (((lane >> 3) & 2) | (lane >> 5))) << 5) + (lane & 1) * 16)
    f32x4 bsum[2];
    u32x4 of[8];
    unsigned gbits;
    const int tt = wid & 3, ch = wid >> 2, tt16 = mb * 4 + tt;
    const unsigned os = WS_O + (unsigned)(tt16 * 16 + ch * 8) * 1024;
    {
        LANE16();
        const int r32 = lane & 31, hh = lane >> 5;
        const unsigned wdo = WDO(), hv = (unsigned)(lane * 16);
        const char* wdb = (const char*)wd + (size_t)wid * 8192 + nb * 128;
        const char* hb = ws + WS_H + (size_t)((2 * mb + (wid >> 2)) * 112 + (wid & 3)) * 1024;
        unsigned dd = lds0 + wid * 1024;
        asm volatile("" : "+s"(dd));
        auto ld_wd = [&](int s) {
            dma16(wdo, wdb + (size_t)s * 65536, dd + (s % 5) * 16384 + 8192); };
        auto ld_h = [&](int s) { dma16_dev(hv, hb + s * 4096, dd + (s % 5) * 16384); };
#pragma unroll
        for (int s = 0; s < 4; ++s) ld_wd(s);
        const int t = wid & 1, c = (wid >> 1) & 1, kh = wid >> 2;
        int ha[2], wa[2];
#pragma unroll
        for (int e = 0; e < 2; ++e) {
            ha[e] = e * 49152 + (t * 4 + 2 * kh) * 1024 + lane * 16;
            wa[e] = e * 49152 + 8192 + kh * 4096 + (4 * hh + q4) * 128 + (((2 * c + (g4 & 1)) ^ ((q4 & 2) | hh)) << 5) + p4 * 8;
            PIN(ha[e]); PIN(wa[e]);
        }
        unsigned hfo = WS_HF + mb * 512 + lane * 8;
        PIN(hfo);
        u64 seen = ~0ull;
        if (wid == 0) seen = flag_wait_some<4>(ws, hfo, tag, 0xffffull);
        if (wid == 1) flag_wait<4>(ws, WS_PF + (mb >> 1) * 128 + r16 * 8, tag);
        const char* wob = (const char*)wo + (size_t)wid * 32768 + nb * 128;
        auto ld_wo = [&](int half) {
#pragma unroll
            for (int e = 0; e < 4; ++e) dma16(wdo, wob + half * 262144 + e * 8192, lds0 + (half ? 0 : 49152) + wid * 4096 + e * 1024);
        };
        BAR();
        STAMP(1, 3);
        gbits = __builtin_amdgcn_raw_buffer_load_b32(rs, WS_G + (unsigned)(tt16 * 16 + r16) * 4, 0, 16);
#pragma unroll
        for (int s = 0; s < 4; ++s) ld_h(s);
        __builtin_amdgcn_s_setprio(F_PRIO_DN);
        f32x16 acc;
#pragma unroll
        for (int i = 0; i < 16; ++i) acc[i] = 0.f;
        bf16x8 hf[2][2], wf[2][2];
        static_for<0, 29>([&](auto S) {
            constexpr int s = decltype(S)::value, p = s & 1, sl = s % 5, e = sl >= 3, so = (sl - 3 * e) * 16384;
            if constexpr (s < 28) {
                if constexpr (s % 7 == 3 && s < 24) {
                    constexpr u64 want = 0xffffull << (16 * ((s + 4) / 7));
                    if ((seen & want) != want) { F_HI(); seen = flag_wait_some<4>(ws, hfo, tag, want); __builtin_amdgcn_s_setprio(F_PRIO_DN); }
                }
                wait_vm_bar<(s < 4 ? 3 + s : (s < 25 ? 6 : (s == 25 ? 4 : 58 - 2 * s)))>();
                if constexpr (s + 4 < 28) { ld_wd(s + 4); ld_h(s + 4); }
                if constexpr (s == 25) ld_wo(0);
                if constexpr (s == 27) {
                    ld_wo(1);
#pragma unroll
                    for (int k = 0; k < 8; ++k) of[k] = ld16_dev(rs, hv, os + k * 1024);
                }
#pragma unroll
                for (int k = 0; k < 2; ++k) {
                    hf[p][k] = LD8(ha[e] + so + k * 1024);
                    wf[p][k] = cat8(tr_read(L + wa[e] + so + k * 2048), tr_read(L + wa[e] + so + k * 2048 + 1024));
                }
            }
            SBAR();
            if constexpr (s > 0) { acc = MFMA32(wf[p ^ 1][0], hf[p ^ 1][0], acc); acc = MFMA32(wf[p ^ 1][1], hf[p ^ 1][1], acc); }
            SBAR();
        });
        F_HI();
        STAMP(1, 4);
        LAS f32x4* red = (LAS f32x4*)(L + 32768 + ((c * 8 + hh) * 64 + t * 32 + r32) * 16);
        BAR();
        if (kh) {
#pragma unroll
            for (int j = 0; j < 4; ++j) red[j * 128] = (f32x4){acc[4 * j], acc[4 * j + 1], acc[4 * j + 2], acc[4 * j + 3]};
        }
        LGKM_BAR();
        if (!kh) {
#pragma unroll
            for (int j = 0; j < 4; ++j) red[j * 128] += (f32x4){acc[4 * j], acc[4 * j + 1], acc[4 * j + 2], acc[4 * j + 3]};
        }
        LGKM_BAR();
        const int ro = 32768 + (((wid >> 2) * 8 + g4) * 64 + (wid & 3) * 16 + r16) * 16;
#pragma unroll
        for (int r = 0; r < 2; ++r) bsum[r] = *(LAS const f32x4*)(L + ro + r * 4096);
        PIN(bsum[0]); PIN(bsum[1]);
    }
    WAIT_VM_BAR(8);
    STAMP(1, 5);
    {
        LANE16();
        int rd[4];
#pragma unroll
        for (int c = 0; c < 4; ++c) { rd[c] = (ch ? 0 : 49152) + (16 * (g4 >> 1) + 4 * (g4 & 1) + q4) * 128 + ((((2 * ch + c) & 3) ^ ((q4 & 2) | (g4 & 1))) << 5) + p4 * 8; PIN(rd[c]); }
        unsigned oo = (unsigned)((tt16 * 16 + r16) * DM + nb * 64 + ch * 32 + (g4 & 1) * 16 + (g4 >> 1) * 8) * 2;
        int xw = 32768 + wid * 2048 + lane * 16, xr = xw ^ 8192;
        PIN(oo); PIN(xw); PIN(xr);
        bf16x8 wf[WFD][4];
        auto rdw = [&](int k) {
#pragma unroll
            for (int c = 0; c < 4; ++c) wf[k % WFD][c] = cat8(tr_read(L + rd[c] + k * 4096), tr_read(L + rd[c] + k * 4096 + 1024));
        };
#pragma unroll
        for (int k = 0; k < WFD; ++k) { rdw(k); PIN(wf[k][0]); PIN(wf[k][1]); PIN(wf[k][2]); PIN(wf[k][3]); }
        const unsigned ov_ = (unsigned)(lane * 16);
        unsigned need = 0xff;
        auto check = [&](int from) {
#pragma unroll
            for (int k = 0; k < 8; ++k)
                if (k >= from && (need >> k & 1)) { if (!__any(max(max(of[k][0], of[k][1]), max(of[k][2], of[k][3])) == EMPTY)) need &= ~(1u << k); }
        };
        check(0);
        const u64 tw = __builtin_amdgcn_s_memrealtime();
        __builtin_amdgcn_s_setprio(O_PRIO);
        auto again = [&](int from) {
            unsigned so;
#define OLD(K, R) "s_bitcmp1_b32 %9, " #K "\n\ts_cbranch_scc0 1f\n\ts_add_u32 %8, %12, " #K "*1024\n\tbuffer_load_dwordx4 " R ", %10, %11, %8 offen sc1\n1:\n\t"
            asm volatile("s_cmp_eq_u32 %9, 0xff\n\ts_cbranch_scc0 1f\n\ts_sleep %13\n1:\n\t" OLD(0, "%0") OLD(1, "%1") OLD(2, "%2") OLD(3, "%3") OLD(4, "%4") OLD(5, "%5") OLD(6, "%6") OLD(7, "%7") "s_waitcnt vmcnt(0)"
                         : "+v"(of[0]), "+v"(of[1]), "+v"(of[2]), "+v"(of[3]), "+v"(of[4]), "+v"(of[5]), "+v"(of[6]), "+v"(of[7]), "=&s"(so)
                         : "s"(need), "v"(ov_), "s"(rs), "s"(os), "n"(O_NAP) : "memory", "scc");
            check(from);
            if (__builtin_amdgcn_s_memrealtime() - tw > 2000000ull) need = 0;
        };
        while (need) again(0);
        STAMP(1, 6);
        const float gt = __uint_as_float(gbits);
        f32x4 ac[4];
#pragma unroll
        for (int c = 0; c < 4; ++c) ac[c] = (f32x4){0.f, 0.f, 0.f, 0.f};
#pragma unroll
        for (int k = 0; k < 8; ++k) {
#pragma unroll
            for (int c = 0; c < 4; ++c) ac[c] = MFMA16(wf[k % WFD][c], BC8(of[k]), ac[c]);
            SBAR();
            if (k + WFD < 8) rdw(k + WFD);
            SBAR();
        }
        *(LAS f32x4*)(L + xw) = ac[2]; *(LAS f32x4*)(L + xw + 1024) = ac[3];
        LGKM_BAR();
        const f32x4 a0 = ac[0] + *(LAS const f32x4*)(L + xr), a1 = ac[1] + *(LAS const f32x4*)(L + xr + 1024);
        const float gb = 1.f - gt;
        const f32x4 r0 = a0 * gt + bsum[0] * gb, r1 = a1 * gt + bsum[1] * gb;
        const auto s0 = __builtin_amdgcn_permlane16_swap(cvtpk(r0[0], r0[1]), cvtpk(r1[0], r1[1]), false, false);
        const auto s1 = __builtin_amdgcn_permlane16_swap(cvtpk(r0[2], r0[3]), cvtpk(r1[2], r1[3]), false, false);
        const u32x4 ov = {s0[0], s1[0], s0[1], s1[1]};
        *(u32x4*)((char*)out + oo) = ov;
    }
    STAMP(1, 7);
}

__global__ __launch_bounds__(512, 4) void fused_fwd(const u16* __restrict__ x, const u16* __restrict__ wqkv, const u16* __restrict__ wo,
                                                     const u16* __restrict__ wg, const u16* __restrict__ wu, const u16* __restrict__ wd,
                                                     const u16* __restrict__ gw, char* __restrict__ ws, u16* __restrict__ out) {
    extern __shared__ __attribute__((aligned(16))) char lds[];
    const char* code;
    asm volatile("s_getpc_b64 %0" : "=s"(code));
    if (blockIdx.x < 256) role_a(blockIdx.x, x, wqkv, gw, wd, wo, ws, lds, code);
    else role_f(blockIdx.x - 256, x, wqkv, wo, wg, wu, wd, ws, out, lds);
}

extern "C" void kernel_launch(void* const* d_in, const int* in_sizes, int n_in, void* d_out, int out_size, void* d_ws, size_t ws_size,
                              hipStream_t stream) {
    (void)hipFuncSetAttribute((const void*)fused_fwd, hipFuncAttributeMaxDynamicSharedMemorySize, LDS_BYTES);
    fused_fwd<<<512, 512, LDS_BYTES, stream>>>((const u16*)d_in[0], (const u16*)d_in[1], (const u16*)d_in[2], (const u16*)d_in[4],
                                               (const u16*)d_in[5], (const u16*)d_in[6], (const u16*)d_in[7], (char*)d_ws, (u16*)d_out);
}
```

```cpp
#include <hip/hip_runtime.h>
#include <hip/hip_bf16.h>
#include <stdint.h>

typedef short bf16x8 __attribute__((ext_vector_type(8)));
typedef short s16x4 __attribute__((ext_vector_type(4)));
typedef float f32x16 __attribute__((ext_vector_type(16)));
typedef float f32x4 __attribute__((ext_vector_type(4)));
typedef float f32x2 __attribute__((ext_vector_type(2)));
typedef unsigned u32x4 __attribute__((ext_vector_type(4)));
typedef unsigned u32x2 __attribute__((ext_vector_type(2)));
typedef __bf16 bf16x2_t __attribute__((ext_vector_type(2)));
typedef unsigned short u16;
typedef unsigned long long u64;
#define LAS __attribute__((address_space(3)))
#define GAS __attribute__((address_space(1)))
#define DI __device__ __forceinline__
#define MFMA32(a, b, c) __builtin_amdgcn_mfma_f32_32x32x16_bf16((a), (b), (c), 0, 0, 0)
#define SBAR() __builtin_amdgcn_sched_barrier(0)
#define PIN(x) asm volatile("" : "+v"(x))
#define BC8(x) __builtin_bit_cast(bf16x8, x)
template <int V> struct IC { static constexpr int value = V; };
template <int I, int N, class F> DI void static_for(F&& f) { if constexpr (I < N) { f(IC<I>{}); static_for<I + 1, N>(f); } }

constexpr int SEQ = 2048, DM = 512, FF = 1792;
constexpr unsigned WS_E = 0;
constexpr unsigned WS_KVF = 4096;
constexpr unsigned WS_HF = WS_KVF + 8192;
constexpr unsigned WS_PF = WS_HF + 16384;
constexpr unsigned WS_G = WS_PF + 4096;
constexpr unsigned WS_KV = 65536;
constexpr unsigned WS_H = WS_KV + (4u << 20);
constexpr unsigned WS_O = WS_H + 128u * 56 * 1024;
constexpr unsigned WS_END = WS_O + 128u * 16 * 1024;
constexpr float QSCALE = 0.25f * 1.4426950408889634f;
constexpr int LDS_BYTES = 81920;
constexpr unsigned CODE_BYTES = 45824;
constexpr unsigned EMPTY = 0xffffffffu;

DI unsigned cvtpk(float lo, float hi) { const f32x2 v = {lo, hi}; return __builtin_bit_cast(unsigned, __builtin_convertvector(v, bf16x2_t)); }
DI s16x4 tr_read(LAS const char* p) { return __builtin_amdgcn_ds_read_tr16_b64_v4i16((LAS s16x4*)p); }
DI bf16x8 cat8(s16x4 a, s16x4 b) { return __builtin_shufflevector(a, b, 0, 1, 2, 3, 4, 5, 6, 7); }
DI void dma16(unsigned voff, const void* sbase, unsigned lds) {
    asm volatile("s_mov_b32 m0, %2\n\ts_nop 0\n\tglobal_load_lds_dwordx4 %0, %1" :: "v"(voff), "s"(sbase), "s"(lds) : "memory");
}
DI void dma16_dev(unsigned voff, const void* sbase, unsigned lds) {
    asm volatile("s_mov_b32 m0, %2\n\ts_nop 0\n\tglobal_load_lds_dwordx4 %0, %1 sc1" :: "v"(voff), "s"(sbase), "s"(lds) : "memory");
}
DI void dma64_dev(unsigned voff, const void* sbase, unsigned lds) {
    asm volatile("s_mov_b32 m0, %2\n\ts_nop 0\n\tglobal_load_lds_dwordx4 %0, %1 sc1\n\tglobal_load_lds_dwordx4 %0, %1 offset:1024 sc1\n\t"
                 "global_load_lds_dwordx4 %0, %1 offset:2048 sc1\n\tglobal_load_lds_dwordx4 %0, %1 offset:3072 sc1" :: "v"(voff), "s"(sbase), "s"(lds) : "memory");
}
DI void dma32_dev(unsigned voff, const void* sbase, unsigned lds) {
    asm volatile("s_mov_b32 m0, %2\n\ts_nop 0\n\tglobal_load_lds_dwordx4 %0, %1 sc1\n\tglobal_load_lds_dwordx4 %0, %1 offset:1024 sc1" :: "v"(voff), "s"(sbase), "s"(lds) : "memory");
}
DI void dma16_48(unsigned voff, const void* sbase, unsigned lds) {
    asm volatile("s_mov_b32 m0, %2\n\ts_mov_b32 exec_hi, 0xffff\n\tglobal_load_lds_dwordx4 %0, %1\n\ts_mov_b32 exec_hi, -1" :: "v"(voff), "s"(sbase), "s"(lds) : "memory");
}
DI void touch(unsigned voff, const void* sbase, unsigned lds) {
    asm volatile("s_mov_b32 m0, %2\n\ts_nop 0\n\tglobal_load_lds_dword %0, %1" :: "v"(voff), "s"(sbase), "s"(lds) : "memory");
}
template <int N> DI void wait_vm_bar() { asm volatile("s_waitcnt vmcnt(%0) lgkmcnt(0)\n\ts_barrier" :: "n"(N) : "memory"); }
#define WAIT_VM(N) asm volatile("s_waitcnt vmcnt(" #N ")" ::: "memory")
#define WAIT_VM_BAR(N) asm volatile("s_waitcnt vmcnt(" #N ") lgkmcnt(0)\n\ts_barrier" ::: "memory")
#define LGKM_BAR() asm volatile("s_waitcnt lgkmcnt(0)\n\ts_barrier" ::: "memory")
#define BAR() asm volatile("s_barrier" ::: "memory")
typedef __amdgpu_buffer_rsrc_t rsrc_t;
DI void st16_dev(rsrc_t r, unsigned off, u32x4 v) { __builtin_amdgcn_raw_buffer_store_b128(v, r, off, 0, 16); }
DI u32x4 ld16_dev(rsrc_t r, unsigned voff, unsigned soff) { return __builtin_amdgcn_raw_buffer_load_b128(r, voff, soff, 16); }
DI void flag_set(char* ws, unsigned off, u64 tag) { __hip_atomic_store((GAS u64*)(ws + off), tag, __ATOMIC_RELAXED, __HIP_MEMORY_SCOPE_AGENT); }
DI u64 flag_get(const char* ws, unsigned off) {
    u64 v;
    asm volatile("global_atomic_or_x2 %0, %1, %3, %2 sc0 sc1\n\ts_waitcnt vmcnt(0)" : "=&v"(v) : "v"(off), "s"(ws), "v"(0ull) : "memory");
    return v;
}
DI u64 flag_peek(const char* ws, unsigned off) { return __hip_atomic_load((const GAS u64*)(ws + off), __ATOMIC_RELAXED, __HIP_MEMORY_SCOPE_AGENT); }
template <int NAP, int LONG_NAP = NAP> DI void flag_wait(const char* ws, unsigned off, u64 tag) {
    const u64 t0 = __builtin_amdgcn_s_memrealtime();
    for (;;) {
        const u64 got = __ballot(flag_peek(ws, off) == tag);
        if (got == ~0ull) break;
        if (__builtin_amdgcn_s_memrealtime() - t0 > 2000000ull) break;
        if (LONG_NAP != NAP && got == 0) __builtin_amdgcn_s_sleep(LONG_NAP); else __builtin_amdgcn_s_sleep(NAP);
    }
    asm volatile("" ::: "memory");
}
template <int NAP> DI u64 flag_wait_some(const char* ws, unsigned off, u64 tag, u64 want) {
    const u64 t0 = __builtin_amdgcn_s_memrealtime();
    u64 got;
    for (;;) {
        got = __ballot(flag_get(ws, off) == tag);
        if ((got & want) == want || __builtin_amdgcn_s_memrealtime() - t0 > 2000000ull) break;
        __builtin_amdgcn_s_sleep(NAP);
    }
    asm volatile("" ::: "memory");
    return got;
}
DI u64 next_tag(u64 e) { return e * 6364136223846793005ull + 1442695040888963407ull; }
DI u64 read_tag(const char* ws) {
    u64 e;
    asm volatile("s_load_dwordx2 %0, %1, 0x0\n\ts_waitcnt lgkmcnt(0)" : "=s"(e) : "s"(ws) : "memory");
    return next_tag(e);
}

DI int fresh_lane() { int l; asm volatile("v_mbcnt_lo_u32_b32 %0, -1, 0\n\tv_mbcnt_hi_u32_b32 %0, -1, %0" : "=v"(l)); return l; }
#define LANE16() const int lane = fresh_lane(), r16 = lane & 15, g4 = lane >> 4, q4 = r16 >> 2, p4 = lane & 3; (void)r16; (void)g4; (void)q4; (void)p4


constexpr int P_W = 49152;
constexpr int P_GW = P_W + 5 * 6144, P_E = P_GW + 1280;
constexpr int A_SLOT = 17408;
constexpr int A_Q = 4 * A_SLOT;

DI f32x16 attn_exact(rsrc_t rs, unsigned kvbase, bf16x8 qf, int lane) {
    const int r32 = lane & 31, hh = lane >> 5;
    f32x16 o, zero;
#pragma unroll
    for (int i = 0; i < 16; ++i) { o[i] = 0.f; zero[i] = 0.f; }
    float m = -INFINITY;
    const unsigned cst = ((r32 & 27) == 16) ? 0x3f803f80u : 0u;
    const u32x4 cv = {cst, cst, cst, cst};
#pragma unroll 1
    for (int t = 0; t < 64; ++t) {
        const unsigned tb = kvbase + (t >> 2) * 16384 + ((t >> 1) & 1) * 4096;
        const bf16x8 kf = BC8(ld16_dev(rs, tb + (t & 1) * 1024 + lane * 16, 0));
        const f32x16 s = MFMA32(kf, qf, zero);
        float tm = s[0];
#pragma unroll
        for (int i = 1; i < 16; ++i) tm = fmaxf(tm, s[i]);
        tm = fmaxf(tm, __shfl_xor(tm, 32));
        const float mn = fmaxf(m, tm);
        const float alpha = __builtin_amdgcn_exp2f(m - mn);
        m = mn;
#pragma unroll
        for (int i = 0; i < 16; ++i) o[i] *= alpha;
#pragma unroll
        for (int st = 0; st < 2; ++st) {
            u32x4 pw;
#pragma unroll
            for (int j = 0; j < 4; ++j) pw[j] = cvtpk(__builtin_amdgcn_exp2f(s[8 * st + 2 * j] - m), __builtin_amdgcn_exp2f(s[8 * st + 2 * j + 1] - m));
            const u32x4 vv = ld16_dev(rs, tb + 8192 + hh * 1024 + ((t & 1) * 2 + st) * 256 + (r32 & 15) * 16, 0);
            o = MFMA32(BC8(r32 < 16 ? vv : cv), BC8(pw), o);
        }
    }
    return o;
}

DI void role_a(int blk, const u16* __restrict__ x, const u16* __restrict__ wqkv, const u16* __restrict__ gw,
               const u16* __restrict__ wd, const u16* __restrict__ wo, const u16* __restrict__ wg, const u16* __restrict__ wu, char* __restrict__ ws, char* lds) {
    const int lane = fresh_lane(), wid = __builtin_amdgcn_readfirstlane(threadIdx.x >> 6);
    const int r32 = lane & 31, hh = lane >> 5;
    const int xcd = blk & 7, bi = blk >> 3;
    const int qb = (xcd & 1) * 8 + (bi & 7), hp = (xcd >> 1) * 4 + (bi >> 3);
    const unsigned lds0 = (unsigned)(uintptr_t)lds;
    LAS char* L = (LAS char*)lds;
    const rsrc_t rs = __builtin_amdgcn_make_buffer_rsrc(ws, 0, WS_END, 0x00020000);
    const unsigned kvb = WS_KV + hp * 262144;
    const unsigned kvown = kvb + qb * 16384;
    float zg = 0.f;
    __builtin_amdgcn_s_setprio(3);
#define LD8(A) (*(LAS const bf16x8*)(L + (A)))
    {
        const int lw = 2 * (wid & 3);
        const int xrow = 8 * lw + (lane >> 3);
        const unsigned xo = (unsigned)(xrow * 1024 + (((lane & 7) ^ ((xrow >> 1) & 7)) << 4)), xo2 = (xo ^ 64) + 8192;
        const unsigned wo_ = (unsigned)((4 * lw + ((lane >> 2) & 3)) * 3072 + (lane >> 4) * 1024 + (lane & 3) * 16);
        const char* xb = (const char*)x + (size_t)qb * 128 * 1024;
        const char* wb = (const char*)wqkv + hp * 64;
        auto ld_x = [&](int h) {
            const char* b = xb + (h & 1) * 65536 + (h >> 1) * 128;
            const unsigned d = lds0 + (((h >> 1) + 1) % 3) * 16384 + (h & 1) * 8192 + lw * 1024;
            dma16(xo, b, d); dma16(xo2, b, d + 1024);
        };
        auto ld_w = [&](int j) {
            const char* b = wb + (size_t)j * (32 * 3072);
            const unsigned d = lds0 + P_W + (j % 5) * 6144 + lw * 768;
            dma16_48(wo_, b, d); dma16_48(wo_, b + 4 * 3072, d + 768);
        };
        if (wid >= 4) {
#pragma unroll
            for (int j = 0; j < 5; ++j) { ld_x(j); ld_w(j); }
            ld_x(5);
        } else {
            if (wid == 3) dma16((unsigned)(lane * 16), gw, lds0 + P_GW);
            else if (wid == 2) touch((unsigned)(lane * 4), ws, lds0 + P_E);
        }
        auto warm_gu = [&](int h) {
            const int l = lane < 56 ? lane : 55, r = (l * 37) >> 8, c = l - 7 * r;
            touch((unsigned)(r * (FF * 2) + c * 128), (const char*)(wid & 1 ? wu : wg) + (size_t)(16 * bi + h * 8) * (FF * 2) + (xcd >> 1) * 896, lds0 + P_GW + 1024);
        };
        auto proj = [&](auto KIND) {
            constexpr int kind = decltype(KIND)::value, NT = kind == 0 ? 1 : 2;
            const int row0 = kind == 0 ? (wid - 4) * 32 : (wid & 1) * 64;
            int xa[4];
#pragma unroll
            for (int k = 0; k < 4; ++k) { xa[k] = ((row0 + r32) * 128 + ((hh ^ ((r32 >> 1) & 7)) << 4)) ^ (k << 5); PIN(xa[k]); }
            const int wa = P_W + hh * 1536 + kind * 256 + ((lane & 15) >> 2) * 64 + ((lane >> 4) & 1) * 32 + (lane & 3) * 8;
            int ga = (8 * hp + (lane >> 3)) * 128 + (((lane & 7) ^ ((4 * hp + (lane >> 4)) & 7)) << 4), gb = P_GW + (lane & 7) * 16;
            if (kind == 2) { PIN(ga); PIN(gb); }
            int eo = kind == 0 ? A_Q + (wid - 4) * 2048 + lane * 16
                               : (wid & 1) * 4096 + (kind == 1 ? lane * 16 : 8192 + (r32 >> 4) * 2048 + hh * 1024 + (r32 & 15) * 16);
            PIN(eo);
            f32x16 acc[NT];
#pragma unroll
            for (int t = 0; t < NT; ++t)
#pragma unroll
                for (int i = 0; i < 16; ++i) acc[t][i] = 0.f;
            bf16x8 xf[2][2][NT], wf[2][2];
            u32x4 gx, gv;
            float z = 0.f;
            static_for<0, 17>([&](auto J) {
                constexpr int j = decltype(J)::value, c = j & 1;
                constexpr int xs = (((j >> 1) + 1) % 3) * 16384, wsl = (j % 5) * 6144;
                if constexpr (j < 16) {
                    constexpr int issued = j < 2 ? 11 : (2 * j + 8 < 32 ? 2 * j + 8 : 32);
                    if (kind == 2 && j == 0 && wid == 3) WAIT_VM(0);
                    wait_vm_bar<kind == 0 ? 2 * (issued - 1 - (c ? 2 * j + 1 : 2 * j + 2)) : (j < 15 || kind == 1 ? 63 : 0)>();
                }
                auto mm = [&](int ks, int t) {
                    if constexpr (j > 0) acc[t] = kind == 2 ? MFMA32(xf[c ^ 1][ks][t], wf[c ^ 1][ks], acc[t]) : MFMA32(wf[c ^ 1][ks], xf[c ^ 1][ks][t], acc[t]);
                    SBAR();
                };
                auto rw = [&](int ks) { if constexpr (j < 16) wf[c][ks] = cat8(tr_read(L + wa + wsl + ks * 3072), tr_read(L + wa + wsl + ks * 3072 + 768)); };
                auto rx = [&](int ks, int t) { if constexpr (j < 16) xf[c][ks][t] = LD8(xa[2 * c + ks] + xs + t * 4096); };
                if constexpr (kind == 0) {
                    rw(0); rx(0, 0); rw(1); rx(1, 0);
                    SBAR();
                    if constexpr (j + 4 < 16 && j >= 2) ld_x(j + 4);
                    mm(0, 0);
                    if constexpr (j + 4 < 16 && j >= 1) ld_w(j + 4);
                    mm(1, 0);
                } else {
                    if constexpr (kind == 1 && j == 12) { warm_gu(0); warm_gu(1); }
                    mm(0, 0); rw(0); rx(0, 0); SBAR();
                    mm(0, 1); rx(0, 1); rw(1); SBAR();
                    mm(1, 0); rx(1, 0); rx(1, 1);
                    if (kind == 2 && j < 16 && c == 0 && wid == 3) { gx = *(LAS const u32x4*)(L + ga + xs); gv = *(LAS const u32x4*)(L + gb + (j >> 1) * 128); }
                    SBAR();
                    mm(1, 1);
                    if (kind == 2 && c == 1 && wid == 3) {
#pragma unroll
                        for (int e = 0; e < 4; ++e) {
                            z = fmaf(__uint_as_float(gx[e] << 16), __uint_as_float(gv[e] << 16), z);
                            z = fmaf(__uint_as_float(gx[e] & 0xffff0000u), __uint_as_float(gv[e] & 0xffff0000u), z);
                        }
                    }
                }
            });
#pragma unroll
            for (int i = 0; i < NT; ++i)
#pragma unroll
                for (int e = 0; e < 2; ++e) {
                    u32x4 v;
#pragma unroll
                    for (int j = 0; j < 4; ++j)
                        v[j] = kind == 0 ? cvtpk(acc[i][8 * e + 2 * j] * QSCALE, acc[i][8 * e + 2 * j + 1] * QSCALE) : cvtpk(acc[i][8 * e + 2 * j], acc[i][8 * e + 2 * j + 1]);
                    const int off = eo + (kind == 0 ? e * 1024 : (kind == 1 ? e * 2048 + i * 1024 : (2 * i + e) * 256));
                    if constexpr (kind > 0) __builtin_amdgcn_raw_buffer_store_b128(v, rs, off, kvown, 16);
                    *(LAS u32x4*)(L + off) = v;
                }
            if (kind == 2) zg = z;
        };
        if (wid >= 4) proj(IC<0>{}); else if (wid < 2) proj(IC<1>{}); else proj(IC<2>{});
    }
    const unsigned cst = ((lane & 11) == 0) ? 0x3f803f80u : 0u;
    if (wid == 4) *(LAS u32x4*)(L + 16384 + lane * 16) = (u32x4){cst, cst, cst, cst};
    LGKM_BAR();
    const u32x2 ew = *(LAS const u32x2*)(L + P_E);
    const u64 tag = next_tag((u64)(unsigned)__builtin_amdgcn_readfirstlane(ew[1]) << 32 | (unsigned)__builtin_amdgcn_readfirstlane(ew[0]));
    const bool mover = wid >= 4;
    const char* kvg = ws + kvb + (wid & 3) * 4096;
    const unsigned doff = (unsigned)(lane * 16);
    auto fetch = [&](int c, int slot) {
        dma64_dev(doff, kvg + (size_t)((qb + c) & 15) * 16384, lds0 + slot * A_SLOT + (wid & 3) * 4096);
    };
    if (wid > 4) *(LAS u32x4*)(L + (wid - 4) * A_SLOT + 16384 + lane * 16) = (u32x4){cst, cst, cst, cst};
    const int hsel = wid & 1, qp = (wid >> 1) & 1, kh = wid >> 2;
    const bf16x8 qA = *(LAS const bf16x8*)(L + A_Q + ((2 * qp) * 2 + hsel) * 1024 + lane * 16);
    const bf16x8 qB = *(LAS const bf16x8*)(L + A_Q + ((2 * qp + 1) * 2 + hsel) * 1024 + lane * 16);
    int ka = kh * 4096 + hsel * 2048 + lane * 16;
    int va = r32 < 16 ? 8192 + kh * 4096 + hsel * 2048 + hh * 1024 + r32 * 16 : 16384 + (r32 & 15) * 16;
    PIN(ka); PIN(va);
    bf16x8 kf = LD8(ka);
    f32x16 negm, oA, oB, sX, sY;
    float mref;
    {
        f32x16 zero;
#pragma unroll
        for (int i = 0; i < 16; ++i) { zero[i] = 0.f; oA[i] = 0.f; oB[i] = 0.f; }
        sX = MFMA32(kf, qA, zero); sY = MFMA32(kf, qB, zero);
        float tm = fmaxf(sX[0], sY[0]);
#pragma unroll
        for (int i = 1; i < 16; ++i) tm = fmaxf(tm, fmaxf(sX[i], sY[i]));
        mref = fmaxf(tm, __shfl_xor(tm, 32));
#pragma unroll
        for (int i = 0; i < 16; ++i) negm[i] = -mref;
        PIN(negm);
    }
    sX = MFMA32(kf, qA, negm);
    bf16x8 v0 = LD8(va), v1 = v0;
    u32x4 p0, p1 = {0u, 0u, 0u, 0u};
    auto publish = [&]() {
        WAIT_VM(0);
        if (fresh_lane() == 0) flag_set(ws, WS_KVF + hp * 512 + (unsigned)(wid * 16 + qb) * 8, tag);
    };
    if (wid < 4) publish();
    if (wid == 3) {
        zg += __shfl_xor(zg, 1); zg += __shfl_xor(zg, 2); zg += __shfl_xor(zg, 4);
        if ((lane & 7) == 0) __builtin_amdgcn_raw_buffer_store_b32(__float_as_uint(1.f / (1.f + __expf(-zg))), rs, WS_G + (unsigned)(qb * 128 + 8 * hp + (lane >> 3)) * 4, 0, 16);
    }
#define EX2(X, i) X[i] = __builtin_amdgcn_exp2f(X[i])
#define HALF(SC, M1, L1, M2, L2, M3, L3) do { \
        EX2(SC, 0); EX2(SC, 1); PIN(SC); SBAR(); \
        M1; SBAR(); L1; \
        EX2(SC, 2); EX2(SC, 3); EX2(SC, 4); EX2(SC, 5); EX2(SC, 6); EX2(SC, 7); PIN(SC); SBAR(); \
        M2; SBAR(); L2; \
        p0[0] = cvtpk(SC[0], SC[1]); p0[1] = cvtpk(SC[2], SC[3]); p0[2] = cvtpk(SC[4], SC[5]); p0[3] = cvtpk(SC[6], SC[7]); PIN(p0); SBAR(); \
        EX2(SC, 8); EX2(SC, 9); EX2(SC, 10); EX2(SC, 11); PIN(SC); SBAR(); \
        M3; SBAR(); L3; \
        EX2(SC, 12); EX2(SC, 13); EX2(SC, 14); EX2(SC, 15); \
        p1[0] = cvtpk(SC[8], SC[9]); p1[1] = cvtpk(SC[10], SC[11]); p1[2] = cvtpk(SC[12], SC[13]); p1[3] = cvtpk(SC[14], SC[15]); PIN(p1); SBAR(); \
    } while (0)
#define PR(V) do { if ((V) >= 0) __builtin_amdgcn_s_setprio((V) < 0 ? 0 : (V)); } while (0)
#define CHUNK(S0, S1, SYNC, Q1, Q3, Q4) do { \
        PR(Q1); \
        HALF(sX, oB = MFMA32(v1, BC8(p1), oB), v1 = LD8(va + (S0) + 256), sY = MFMA32(kf, qB, negm), kf = LD8(ka + (S0) + 1024), oA = MFMA32(v0, BC8(p0), oA), (void)0); \
        HALF(sY, oA = MFMA32(v1, BC8(p1), oA), (void)0, sX = MFMA32(kf, qA, negm), (void)0, oB = MFMA32(v0, BC8(p0), oB), v0 = LD8(va + (S0) + 512)); \
        PR(Q3); \
        HALF(sX, oB = MFMA32(v1, BC8(p1), oB), v1 = LD8(va + (S0) + 768), sY = MFMA32(kf, qB, negm), SYNC; kf = LD8(ka + (S1)), oA = MFMA32(v0, BC8(p0), oA), (void)0); \
        PR(Q4); \
        HALF(sY, oA = MFMA32(v1, BC8(p1), oA), (void)0, sX = MFMA32(kf, qA, negm), (void)0, oB = MFMA32(v0, BC8(p0), oB), v0 = LD8(va + (S1))); \
    } while (0)
    auto warm = [&](int q) {
        const int idx = bi * 2 + (wid & 3);
        if ((wid & 3) < 2 && q >= 2 && q <= 6 && (q == 6 || idx < 56))
            touch(doff * 8, (q == 6 ? (const char*)wo : (const char*)wd + (q - 2) * 458752) + idx * 8192, lds0 + A_Q + wid * 256);
    };
    auto late = [&](int q) {
        if (q == 1) {
#pragma unroll
            for (int e = 0; e < 2; ++e)
                __builtin_amdgcn_raw_buffer_store_b128((u32x4){EMPTY, EMPTY, EMPTY, EMPTY}, rs, doff, WS_O + (unsigned)((4 * qb + (wid & 3)) * 32 + 2 * hp + e) * 1024, 16);
        }
        if (q == 2 && (wid & 3) == 3 && fresh_lane() == 0) flag_set(ws, WS_PF + qb * 128 + hp * 8, tag);
    };
#pragma unroll
    for (int n = 0; n < 4; ++n) {
        auto sync_a = [&]() {
            if (n == 0) {
                if (wid == 0) flag_wait_some<3>(ws, WS_KVF + hp * 512 + (unsigned)fresh_lane() * 8, tag, ~0ull);
                BAR();
#pragma unroll
                for (int c = 1; c < 3; ++c) dma32_dev(doff, ws + kvb + ((qb + c) & 15) * 16384 + wid * 2048, lds0 + c * A_SLOT + wid * 2048);
                __builtin_amdgcn_s_setprio(0);
            }
            if (n == 0) WAIT_VM_BAR(2); else
            WAIT_VM_BAR(0);
            PR(3);
            if (mover) { fetch(4 * n + 3, 3); if (n < 3) fetch(4 * n + 4, 0); }
            else { warm(2 * n); late(2 * n); }
        };
        auto sync_b = [&]() {
            WAIT_VM_BAR(0);
            PR(3);
            if (mover) { if (n < 3) { fetch(4 * n + 5, 1); fetch(4 * n + 6, 2); } }
            else { warm(2 * n + 1); late(2 * n + 1); }
        };
        if (n == 0) {
            HALF(sX, oB = MFMA32(v1, BC8(p1), oB), v1 = LD8(va + 256), sY = MFMA32(kf, qB, negm), kf = LD8(ka + 1024), oA = MFMA32(v0, BC8(p0), oA), (void)0);
            HALF(sY, oA = MFMA32(v1, BC8(p1), oA), (void)0, sX = MFMA32(kf, qA, negm), (void)0, oB = MFMA32(v0, BC8(p0), oB), v0 = LD8(va + 512));
            HALF(sX, oB = MFMA32(v1, BC8(p1), oB), v1 = LD8(va + 768), sY = MFMA32(kf, qB, negm), (void)0, oA = MFMA32(v0, BC8(p0), oA), (void)0);
            HALF(sY, oA = MFMA32(v1, BC8(p1), oA), (void)0, (void)0, (void)0, oB = MFMA32(v0, BC8(p0), oB), (void)0);
            sync_a();
            kf = LD8(ka + A_SLOT); v0 = LD8(va + A_SLOT);
            sX = MFMA32(kf, qA, negm);
            SBAR();
        } else
        CHUNK(0, A_SLOT, sync_a(), 0, -1, -1);
        auto sync_1 = [&]() { if (n == 0) { if (!mover) WAIT_VM(0); WAIT_VM_BAR(8); } };
        CHUNK(A_SLOT, 2 * A_SLOT, sync_1(), 2, 1, -1);
        CHUNK(2 * A_SLOT, 3 * A_SLOT, sync_b(), 0, -1, -1);
        if (n < 3) CHUNK(3 * A_SLOT, 0, (void)0, 2, 1, -1); else CHUNK(3 * A_SLOT, 0, (void)0, 2, 1, 0);
    }
    oB = MFMA32(v1, BC8(p1), oB);
    {
        LAS float* ex = (LAS float*)(L + A_SLOT + wid * 2560) + lane;
#pragma unroll
        for (int i = 0; i < 9; ++i) ex[i * 64] = kh ? oA[i] : oB[i];
        ex[9 * 64] = mref;
    }
    LGKM_BAR();
    f32x16 o;
    {
        LAS const float* ex = (LAS const float*)(L + A_SLOT + (wid ^ 4) * 2560) + lane;
        const float mo = ex[9 * 64], mm = fmaxf(mref, mo);
        const float f1 = __builtin_amdgcn_exp2f(mref - mm), f2 = __builtin_amdgcn_exp2f(mo - mm);
#pragma unroll
        for (int i = 0; i < 9; ++i) o[i] = (kh ? oB[i] : oA[i]) * f1 + ex[i * 64] * f2;
        bool ok = (kh ? oB[8] : oA[8]) < 1.0995116e12f && ex[8 * 64] < 1.0995116e12f && o[8] > 9.0949470e-13f;
#pragma unroll
        for (int i = 0; i < 8; ++i) ok = ok && (fabsf(o[i]) < 3.0e38f);
        if (__builtin_expect(!__all(ok), 0)) o = attn_exact(rs, kvb + hsel * 2048, kh ? qB : qA, lane);
    }
    {
        const float inv = 1.f / o[8];
        u32x4 w;
#pragma unroll
        for (int j = 0; j < 4; ++j) w[j] = min(cvtpk(o[2 * j] * inv, o[2 * j + 1] * inv), EMPTY - 1);
        __builtin_amdgcn_raw_buffer_store_b128(w, rs, doff, WS_O + (unsigned)((4 * qb + 2 * qp + kh) * 32 + 2 * hp + hsel) * 1024, 16);
    }
    if (blk == 0 && wid == 7) {
        const u64 te = __builtin_amdgcn_s_memrealtime();
        for (;;) {
            bool ok = true;
#pragma unroll
            for (int i = 0; i < 4; ++i) {
                const int w = i * 64 + lane;
                ok = ok & (flag_peek(ws, WS_KVF + (w >> 4) * 512 + (w & 15) * 8) == tag) & (flag_peek(ws, WS_HF + (w >> 4) * 1024 + (w & 15) * 32) == tag);
            }
            if (__all(ok) || __builtin_amdgcn_s_memrealtime() - te > 2000000ull) break;
            __builtin_amdgcn_s_sleep(8);
        }
        if (lane == 0) flag_set(ws, WS_E, tag);
    }
}

#define F_HI() __builtin_amdgcn_s_setprio(3)
#define F_LO() __builtin_amdgcn_s_setprio(0)
DI void role_f(int blk, const u16* __restrict__ x, const u16* __restrict__ wqkv, const u16* __restrict__ wo, const u16* __restrict__ wg, const u16* __restrict__ wu,
               const u16* __restrict__ wd, char* __restrict__ ws, u16* __restrict__ out, char* lds) {
    const int wid = __builtin_amdgcn_readfirstlane(threadIdx.x >> 6);
    const int xcd = blk & 7, bi = blk >> 3;
    const int cq = xcd >> 1, rb = (xcd & 1) * 8 + (bi & 7), cb = cq * 4 + (bi >> 3);
    const int mb = 4 * xcd + (bi >> 3), nb = bi & 7;
    const unsigned lds0 = (unsigned)(uintptr_t)lds;
    LAS char* L = (LAS char*)lds;
    const rsrc_t rs = __builtin_amdgcn_make_buffer_rsrc(ws, 0, WS_END, 0x00020000);
    const u64 tstart = __builtin_amdgcn_s_memrealtime();
    auto wait_until = [&](unsigned ticks) { while ((unsigned)(__builtin_amdgcn_s_memrealtime() - tstart) < ticks) __builtin_amdgcn_s_sleep(2); };
    F_HI();
    {
        const int lane = fresh_lane(), idx = bi * 8 + wid;
        __builtin_amdgcn_s_sleep(4);
        const int xl = lane < 20 ? lane : 19;
        touch((unsigned)(idx * 4096 + (xl & 3) * 1024 + (3 + (xl >> 2)) * 128), (const char*)x + (size_t)(xcd & 1) * (1 << 20), lds0 + wid * 1024);
        const int q = lane < 12 ? lane : 11, qr = q >= 6, qc = q - 6 * qr;
        if (idx >= 80) touch((unsigned)((2 * idx + qr) * 3072 + (qc >> 1) * 1024 + (qc & 1) * 128), (const char*)wqkv + cq * 256, lds0 + wid * 1024);
    }
    u64 tag;
    {
        LANE16();
        const int r32 = lane & 31, hh = lane >> 5;
        const int xrow = 8 * wid + (lane >> 3);
        unsigned xo = (unsigned)(xrow * 1024 + (((lane & 7) ^ ((xrow >> 1) & 7)) << 4));
        const int bg = (r16 >> 1) ^ (2 * g4), bu = bg ^ 1;
        const unsigned wrow = (unsigned)((4 * wid + g4) * (FF * 2) + (lane & 1) * 16);
        const int odd = cb & 1;
        unsigned go = wrow + bg * 32, uo = wrow + bu * 32;
        PIN(xo); PIN(go); PIN(uo);
        const char* xb = (const char*)x + (size_t)rb * 128 * 1024;
        const char* gb = (const char*)wg + cb * 224 - odd * 32;
        const char* ub = (const char*)wu + cb * 224 - odd * 32;
        const unsigned dw = lds0 + wid * 1024;
        auto ld_x = [&](int s) { const unsigned d = dw + (s & 1) * 16384; dma16(xo, xb + s * 128, d); dma16(xo, xb + 65536 + s * 128, d + 8192); };
        auto ld_w = [&](int j) {
            const unsigned d = dw + 32768 + (j % 3) * 16384; const size_t o = (size_t)j * (32 * FF * 2); dma16(go, gb + o, d); dma16(uo, ub + o, d + 8192); };
        auto gu = [&](auto KIND) {
            constexpr int NT = decltype(KIND)::value, NU = 4 - NT;
            const int t0 = NT == 2 ? (wid & 1) * 2 : wid - 4, u0 = NT == 2 ? (wid >> 1) * 2 : 4;
            LAS const char *xa[4], *wa[NU];
#pragma unroll
            for (int k = 0; k < 4; ++k) { xa[k] = L + (((t0 * 32 + r32) * 128 + ((hh ^ ((r32 >> 1) & 7)) << 4)) ^ (k << 5)); PIN(xa[k]); }
#pragma unroll
            for (int u = 0; u < NU; ++u) { wa[u] = L + 32768 + (g4 & 1) * 8192 + (8 * hh + q4) * 256 + (((u0 + u + odd) ^ (2 * q4 + (g4 & 1))) << 5) + p4 * 8; PIN(wa[u]); }
            f32x16 acc[NT][NU];
#pragma unroll
            for (int t = 0; t < NT; ++t)
#pragma unroll
                for (int u = 0; u < NU; ++u)
#pragma unroll
                    for (int i = 0; i < 16; ++i) acc[t][u][i] = 0.f;
            bf16x8 x0[NT], w0[NU], x1[2][NT], w1[2][NU];
            auto rd = [&](auto J, int ks, bf16x8 (&xf)[NT], bf16x8 (&wf)[NU]) {
                constexpr int j = decltype(J)::value;
#pragma unroll
                for (int t = 0; t < NT; ++t) xf[t] = *(LAS const bf16x8*)(xa[2 * (j & 1) + ks] + ((j >> 1) & 1) * 16384 + t * 4096);
#pragma unroll
                for (int u = 0; u < NU; ++u) { LAS const char* a = wa[u] + (j % 3) * 16384 + ks * 4096; wf[u] = cat8(tr_read(a), tr_read(a + 1024)); }
            };
            auto mm = [&](bf16x8 (&xf)[NT], bf16x8 (&wf)[NU]) {
#pragma unroll
                for (int t = 0; t < NT; ++t)
#pragma unroll
                    for (int u = 0; u < NU; ++u) acc[t][u] = MFMA32(wf[u], xf[t], acc[t][u]);
            };
            wait_until(300);
            tag = read_tag(ws);
            wait_until(600);
            ld_x(0); ld_w(0);
            BAR();
            ld_w(1); ld_x(1); ld_w(2);
            F_LO();
            WAIT_VM_BAR(6);
            rd(IC<0>{}, 0, x0, w0); rd(IC<0>{}, 1, x1[0], w1[0]);
            static_for<0, 16>([&](auto J) {
                constexpr int j = decltype(J)::value, c = j & 1;
                wait_vm_bar<j >= 14 ? 0 : (c ? 2 : 4)>();
                if constexpr (c == 0) { if constexpr (j + 3 < 16) ld_w(j + 3); }
                else if constexpr (j + 3 < 16) { ld_x((j + 3) >> 1); ld_w(j + 3); }
                if constexpr (j < 15) rd(IC<j + 1>{}, 1, x1[c ^ 1], w1[c ^ 1]);
                SBAR();
                mm(x0, w0);
                SBAR();
                if constexpr (j < 15) rd(IC<j + 1>{}, 0, x0, w0);
                SBAR();
                mm(x1[c], w1[c]);
                SBAR();
            });
            F_HI();
            const int lane = fresh_lane();
#pragma unroll
            for (int t = 0; t < NT; ++t)
#pragma unroll
                for (int u = 0; u < NU; ++u) {
                    u32x4 v;
#pragma unroll
                    for (int j = 0; j < 4; ++j) v[j] = cvtpk(acc[t][u][2 * j] * acc[t][u][8 + 2 * j], acc[t][u][2 * j + 1] * acc[t][u][9 + 2 * j]);
                    st16_dev(rs, WS_H + (unsigned)((rb * 4 + t0 + t) * 112 + 7 * cb + u0 + u) * 1024 + lane * 16, v);
                }
            WAIT_VM(0);
            if (lane == 0) flag_set(ws, WS_HF + (2 * rb + (t0 >> 1)) * 512 + (unsigned)(cb * 4 + (NT == 2 ? wid >> 1 : 2 + (wid & 1))) * 8, tag);
        };
        if (wid < 4) gu(IC<2>{}); else gu(IC<1>{});
    }
#define WDO() (unsigned)((lane >> 3) * 1024 + ((((lane & 7) >> 1) ^ (((lane >> 3) & 2) | (lane >> 5))) << 5) + (lane & 1) * 16)
    {
        LANE16();
        const int r32 = lane & 31, hh = lane >> 5;
        const unsigned wdo = WDO(), hv = (unsigned)(lane * 16);
        const char* wdb = (const char*)wd + (size_t)wid * 8192 + nb * 128;
        const char* hb = ws + WS_H + (size_t)((2 * mb + (wid >> 2)) * 112 + (wid & 3)) * 1024;
        unsigned dd = lds0 + wid * 1024;
        asm volatile("" : "+s"(dd));
        auto ld_wd = [&](int s) {
            dma16(wdo, wdb + (size_t)s * 65536, dd + (s % 5) * 16384 + 8192); };
        auto ld_h = [&](int s) { dma16_dev(hv, hb + s * 4096, dd + (s % 5) * 16384); };
#pragma unroll
        for (int s = 0; s < 4; ++s) ld_wd(s);
        const int t = wid & 1, c = (wid >> 1) & 1, kh = wid >> 2;
        LAS const char *ha[2], *wa[2];
        LAS f32x4* red = (LAS f32x4*)(L + 32768 + ((c * 8 + hh) * 64 + t * 32 + r32) * 16);
        PIN(red);
#pragma unroll
        for (int e = 0; e < 2; ++e) {
            ha[e] = L + e * 49152 + (t * 4 + 2 * kh) * 1024 + lane * 16;
            wa[e] = L + e * 49152 + 8192 + kh * 4096 + (4 * hh + q4) * 128 + (((2 * c + (g4 & 1)) ^ ((q4 & 2) | hh)) << 5) + p4 * 8;
            PIN(ha[e]); PIN(wa[e]);
        }
        unsigned hfo = WS_HF + mb * 512 + lane * 8;
        LAS const char* rd = wa[0] - 8192 - kh * 4096 + (kh ? 0 : 49152);
        unsigned oo = (unsigned)((mb * 64 + t * 32 + r32) * DM + nb * 64 + c * 32 + (2 * kh + hh) * 8) * 2;
        PIN(hfo); PIN(rd); PIN(oo);
        u32x4 of[16];
        const unsigned os = WS_O + (unsigned)((2 * mb + t) * 32 + 16 * kh) * 1024;
        u64 seen = ~0ull;
        if (wid == 0) seen = flag_wait_some<4>(ws, hfo, tag, 0xffffull);
        if (wid == 1) flag_wait<4>(ws, WS_PF + (mb >> 1) * 128 + r16 * 8, tag);
        const char* wob = (const char*)wo + (size_t)wid * 32768 + nb * 128;
        auto ld_wo = [&](int half) {
#pragma unroll
            for (int e = 0; e < 4; ++e) dma16(wdo, wob + half * 262144 + e * 8192, lds0 + (half ? 0 : 49152) + wid * 4096 + e * 1024);
        };
        BAR();
        const unsigned gbits = __builtin_amdgcn_raw_buffer_load_b32(rs, WS_G + (unsigned)(mb * 64 + t * 32 + r32) * 4, 0, 16);
#pragma unroll
        for (int s = 0; s < 4; ++s) ld_h(s);
        F_LO();
        f32x16 acc;
#pragma unroll
        for (int i = 0; i < 16; ++i) acc[i] = 0.f;
        bf16x8 hf[2][2], wf[2][2];
        static_for<0, 29>([&](auto S) {
            constexpr int s = decltype(S)::value, p = s & 1, sl = s % 5, e = sl >= 3, so = (sl - 3 * e) * 16384;
            if constexpr (s < 28) {
                if constexpr (s % 7 == 3 && s < 24) {
                    constexpr u64 want = 0xffffull << (16 * ((s + 4) / 7));
                    unsigned part = (unsigned)(seen >> (16 * ((s + 4) / 7)));
                    asm volatile("" : "+s"(part));
                    if ((part & 0xffffu) != 0xffffu) { F_HI(); seen = flag_wait_some<4>(ws, hfo, tag, want); F_LO(); }
                }
                wait_vm_bar<(s < 4 ? 3 + s : (s < 25 ? 6 : (s == 25 ? 4 : 58 - 2 * s)))>();
                if constexpr (s + 4 < 28) { ld_wd(s + 4); ld_h(s + 4); }
                if constexpr (s == 25) ld_wo(0);
                if constexpr (s == 27) ld_wo(1);
#pragma unroll
                for (int k = 0; k < 16; ++k) if (s == 27) of[k] = ld16_dev(rs, hv, os + k * 1024);
#pragma unroll
                for (int k = 0; k < 2; ++k) {
                    hf[p][k] = *(LAS const bf16x8*)(ha[e] + so + k * 1024);
                    wf[p][k] = cat8(tr_read(wa[e] + so + k * 2048), tr_read(wa[e] + so + k * 2048 + 1024));
                }
            }
            SBAR();
            if constexpr (s == 28) F_HI();
            if constexpr (s > 1 && s < 28) PIN(acc);
            if constexpr (s > 0) { acc = MFMA32(wf[p ^ 1][0], hf[p ^ 1][0], acc); acc = MFMA32(wf[p ^ 1][1], hf[p ^ 1][1], acc); }
            SBAR();
        });
        const float gt = __uint_as_float(gbits);
        acc *= 1.f - gt;
        PIN(acc);
        WAIT_VM_BAR(16);
        constexpr int WFD = 4;
        bf16x8 wq[WFD];
        auto rdw = [&](int k) { wq[k % WFD] = cat8(tr_read(rd + k * 2048), tr_read(rd + k * 2048 + 1024)); };
#pragma unroll
        for (int k = 0; k < WFD; ++k) rdw(k);
        SBAR();
        unsigned need = 0xffff;
        auto check = [&](unsigned which) {
#pragma unroll
            for (int k = 0; k < 16; ++k)
                if (which >> k & 1) { if (!__any(max(max(of[k][0], of[k][1]), max(of[k][2], of[k][3])) == EMPTY)) need &= ~(1u << k); }
        };
        {
            unsigned m = 0;
#pragma unroll
            for (int k = 0; k < 16; ++k) m = max(max(max(max(m, of[k][0]), of[k][1]), of[k][2]), of[k][3]);
            PIN(m);
            if (__builtin_expect(__any(m == EMPTY), 0)) check(0xffff); else need = 0;
        }
        SBAR();
#pragma unroll
        for (int k = 0; k < WFD; ++k) PIN(wq[k]);
        if (need) {
            const u64 tw = __builtin_amdgcn_s_memrealtime();
            F_LO();
#define OLD(K, R) "s_bitcmp1_b32 %10, " #K "\n\ts_cbranch_scc0 1f\n\ts_add_u32 %9, %13, " #K "*1024\n\tbuffer_load_dwordx4 " R ", %11, %12, %9 offen sc1\n1:\n\t"
            const unsigned pv_ = hv * 64;
            unsigned pend = 0;
            do {
                unsigned so, v = EMPTY;
                if (__builtin_popcount(need) <= 8) pend = need;
                asm volatile("s_cmp_lg_u32 %10, 0\n\ts_cbranch_scc1 1f\n\ts_sleep %16\n1:\n\t"
                             OLD(0, "%0") OLD(1, "%1") OLD(2, "%2") OLD(3, "%3") OLD(4, "%4") OLD(5, "%5") OLD(6, "%6") OLD(7, "%7")
                             "s_cmp_eq_u32 %14, 0\n\ts_cbranch_scc1 1f\n\ts_mov_b32 exec_lo, %14\n\ts_mov_b32 exec_hi, 0\n\t"
                             "buffer_load_dword %8, %15, %12, %13 offen sc1\n\ts_mov_b64 exec, -1\n1:"
                             : "+v"(of[0]), "+v"(of[1]), "+v"(of[2]), "+v"(of[3]), "+v"(of[4]), "+v"(of[5]), "+v"(of[6]), "+v"(of[7]), "+v"(v), "=&s"(so)
                             : "s"(pend), "v"(hv), "s"(rs), "s"(os), "s"(need & ~pend), "v"(pv_), "n"(4) : "memory", "scc");
                asm volatile(OLD(8, "%0") OLD(9, "%1") OLD(10, "%2") OLD(11, "%3") OLD(12, "%4") OLD(13, "%5") OLD(14, "%6") OLD(15, "%7") "s_waitcnt vmcnt(0)"
                             : "+v"(of[8]), "+v"(of[9]), "+v"(of[10]), "+v"(of[11]), "+v"(of[12]), "+v"(of[13]), "+v"(of[14]), "+v"(of[15]), "+v"(v), "=&s"(so)
                             : "s"(pend), "v"(hv), "s"(rs), "s"(os) : "memory", "scc");
#pragma unroll
                for (int k = 0; k < 8; ++k) PIN(of[k]);
                check(pend);
                pend = (pend | (unsigned)__ballot(v != EMPTY)) & need;
            } while (need && __builtin_amdgcn_s_memrealtime() - tw < 2000000ull);
        }
        f32x16 ao;
#pragma unroll
        for (int i = 0; i < 16; ++i) ao[i] = 0.f;
#pragma unroll
        for (int k = 0; k < 16; ++k) {
            ao = MFMA32(wq[k % WFD], BC8(of[k]), ao);
            SBAR();
            if (k + WFD < 16) rdw(k + WFD);
            SBAR();
        }
        acc += ao * gt;
        auto fin = [&](auto KH) {
            constexpr int a = 2 * decltype(KH)::value, b = 2 - a;
#pragma unroll
            for (int j = b; j < b + 2; ++j) red[j * 128] = (f32x4){acc[4 * j], acc[4 * j + 1], acc[4 * j + 2], acc[4 * j + 3]};
            LGKM_BAR();
            const f32x4 r0 = red[a * 128] + (f32x4){acc[4 * a], acc[4 * a + 1], acc[4 * a + 2], acc[4 * a + 3]};
            const f32x4 r1 = red[a * 128 + 128] + (f32x4){acc[4 * a + 4], acc[4 * a + 5], acc[4 * a + 6], acc[4 * a + 7]};
            const auto s0 = __builtin_amdgcn_permlane32_swap(cvtpk(r0[0], r0[1]), cvtpk(r1[0], r1[1]), false, false);
            const auto s1 = __builtin_amdgcn_permlane32_swap(cvtpk(r0[2], r0[3]), cvtpk(r1[2], r1[3]), false, false);
            *(u32x4*)((char*)out + oo) = (u32x4){s0[0], s1[0], s0[1], s1[1]};
        };
        if (kh) fin(IC<1>{}); else fin(IC<0>{});
    }
}

__global__ __launch_bounds__(512, 4) void fused_fwd(const u16* __restrict__ x, const u16* __restrict__ wqkv, const u16* __restrict__ wo,
                                                     const u16* __restrict__ wg, const u16* __restrict__ wu, const u16* __restrict__ wd,
                                                     const u16* __restrict__ gw, char* __restrict__ ws, u16* __restrict__ out) {
    extern __shared__ __attribute__((aligned(16))) char lds[];
    const char* code;
    asm volatile("s_getpc_b64 %0" : "=s"(code));
    if (blockIdx.x < 24 && threadIdx.x < 128) {
        const unsigned o = ((blockIdx.x >> 3) * 128 + threadIdx.x) * 128;
        touch(min(o, CODE_BYTES), code, (unsigned)(uintptr_t)lds + P_GW + 1024);
    }
    if (__builtin_expect(blockIdx.x >= 256, 0)) role_f(blockIdx.x - 256, x, wqkv, wo, wg, wu, wd, ws, out, lds);
    else role_a(blockIdx.x, x, wqkv, gw, wd, wo, wg, wu, ws, lds);
}

extern "C" void kernel_launch(void* const* d_in, const int* in_sizes, int n_in, void* d_out, int out_size, void* d_ws, size_t ws_size,
                              hipStream_t stream) {
    (void)hipFuncSetAttribute((const void*)fused_fwd, hipFuncAttributeMaxDynamicSharedMemorySize, LDS_BYTES);
    fused_fwd<<<512, 512, LDS_BYTES, stream>>>((const u16*)d_in[0], (const u16*)d_in[1], (const u16*)d_in[2], (const u16*)d_in[4],
                                               (const u16*)d_in[5], (const u16*)d_in[6], (const u16*)d_in[7], (char*)d_ws, (u16*)d_out);
}
```

```cpp
#include <hip/hip_runtime.h>
#include <hip/hip_bf16.h>
#include <stdint.h>

typedef short bf16x8 __attribute__((ext_vector_type(8)));
typedef short s16x4 __attribute__((ext_vector_type(4)));
typedef float f32x16 __attribute__((ext_vector_type(16)));
typedef float f32x4 __attribute__((ext_vector_type(4)));
typedef float f32x2 __attribute__((ext_vector_type(2)));
typedef unsigned u32x4 __attribute__((ext_vector_type(4)));
typedef unsigned u32x2 __attribute__((ext_vector_type(2)));
typedef __bf16 bf16x2_t __attribute__((ext_vector_type(2)));
typedef unsigned short u16;
typedef unsigned long long u64;
#define LAS __attribute__((address_space(3)))
#define GAS __attribute__((address_space(1)))
#define DI __device__ __forceinline__
#define MFMA32(a, b, c) __builtin_amdgcn_mfma_f32_32x32x16_bf16((a), (b), (c), 0, 0, 0)
#define SBAR() __builtin_amdgcn_sched_barrier(0)
#define PIN(x) asm volatile("" : "+v"(x))
#define BC8(x) __builtin_bit_cast(bf16x8, x)
template <int V> struct IC { static constexpr int value = V; };
template <int I, int N, class F> DI void static_for(F&& f) { if constexpr (I < N) { f(IC<I>{}); static_for<I + 1, N>(f); } }

constexpr int SEQ = 2048, DM = 512, FF = 1792;
constexpr unsigned WS_E = 0;
constexpr unsigned WS_KVF = 4096;
constexpr unsigned WS_HF = WS_KVF + 8192;
constexpr unsigned WS_PF = WS_HF + 16384;
constexpr unsigned WS_G = WS_PF + 4096;
constexpr unsigned WS_KV = 65536;
constexpr unsigned WS_H = WS_KV + (4u << 20);
constexpr unsigned WS_O = WS_H + 128u * 56 * 1024;
constexpr unsigned WS_END = WS_O + 128u * 16 * 1024;
constexpr float QSCALE = 0.25f * 1.4426950408889634f;
constexpr int LDS_BYTES = 81920;
constexpr unsigned CODE_BYTES = 46336;
constexpr unsigned EMPTY = 0xffffffffu;

DI unsigned cvtpk(float lo, float hi) { const f32x2 v = {lo, hi}; return __builtin_bit_cast(unsigned, __builtin_convertvector(v, bf16x2_t)); }
DI s16x4 tr_read(LAS const char* p) { return __builtin_amdgcn_ds_read_tr16_b64_v4i16((LAS s16x4*)p); }
DI bf16x8 cat8(s16x4 a, s16x4 b) { return __builtin_shufflevector(a, b, 0, 1, 2, 3, 4, 5, 6, 7); }
DI void dma16(unsigned voff, const void* sbase, unsigned lds) {
    asm volatile("s_mov_b32 m0, %2\n\ts_nop 0\n\tglobal_load_lds_dwordx4 %0, %1" :: "v"(voff), "s"(sbase), "s"(lds) : "memory");
}
DI void dma16_dev(unsigned voff, const void* sbase, unsigned lds) {
    asm volatile("s_mov_b32 m0, %2\n\ts_nop 0\n\tglobal_load_lds_dwordx4 %0, %1 sc1" :: "v"(voff), "s"(sbase), "s"(lds) : "memory");
}
DI void dma64_dev(unsigned voff, const void* sbase, unsigned lds) {
    asm volatile("s_mov_b32 m0, %2\n\ts_nop 0\n\tglobal_load_lds_dwordx4 %0, %1 sc1\n\tglobal_load_lds_dwordx4 %0, %1 offset:1024 sc1\n\t"
                 "global_load_lds_dwordx4 %0, %1 offset:2048 sc1\n\tglobal_load_lds_dwordx4 %0, %1 offset:3072 sc1" :: "v"(voff), "s"(sbase), "s"(lds) : "memory");
}
DI void dma32_dev(unsigned voff, const void* sbase, unsigned lds) {
    asm volatile("s_mov_b32 m0, %2\n\ts_nop 0\n\tglobal_load_lds_dwordx4 %0, %1 sc1\n\tglobal_load_lds_dwordx4 %0, %1 offset:1024 sc1" :: "v"(voff), "s"(sbase), "s"(lds) : "memory");
}
DI void dma16_48(unsigned voff, const void* sbase, unsigned lds) {
    asm volatile("s_mov_b32 m0, %2\n\ts_mov_b32 exec_hi, 0xffff\n\tglobal_load_lds_dwordx4 %0, %1\n\ts_mov_b32 exec_hi, -1" :: "v"(voff), "s"(sbase), "s"(lds) : "memory");
}
DI void touch(unsigned voff, const void* sbase, unsigned lds) {
    asm volatile("s_mov_b32 m0, %2\n\ts_nop 0\n\tglobal_load_lds_dword %0, %1" :: "v"(voff), "s"(sbase), "s"(lds) : "memory");
}
template <int N> DI void wait_vm_bar() { asm volatile("s_waitcnt vmcnt(%0) lgkmcnt(0)\n\ts_barrier" :: "n"(N) : "memory"); }
#define WAIT_VM(N) asm volatile("s_waitcnt vmcnt(" #N ")" ::: "memory")
#define WAIT_VM_BAR(N) asm volatile("s_waitcnt vmcnt(" #N ") lgkmcnt(0)\n\ts_barrier" ::: "memory")
#define LGKM_BAR() asm volatile("s_waitcnt lgkmcnt(0)\n\ts_barrier" ::: "memory")
#define BAR() asm volatile("s_barrier" ::: "memory")
typedef __amdgpu_buffer_rsrc_t rsrc_t;
DI void st16_dev(rsrc_t r, unsigned off, u32x4 v) { __builtin_amdgcn_raw_buffer_store_b128(v, r, off, 0, 16); }
DI u32x4 ld16_dev(rsrc_t r, unsigned voff, unsigned soff) { return __builtin_amdgcn_raw_buffer_load_b128(r, voff, soff, 16); }
DI void flag_set(char* ws, unsigned off, u64 tag) { __hip_atomic_store((GAS u64*)(ws + off), tag, __ATOMIC_RELAXED, __HIP_MEMORY_SCOPE_AGENT); }
DI u64 flag_get(const char* ws, unsigned off) {
    u64 v;
    asm volatile("global_atomic_or_x2 %0, %1, %3, %2 sc0 sc1\n\ts_waitcnt vmcnt(0)" : "=&v"(v) : "v"(off), "s"(ws), "v"(0ull) : "memory");
    return v;
}
DI u64 flag_peek(const char* ws, unsigned off) { return __hip_atomic_load((const GAS u64*)(ws + off), __ATOMIC_RELAXED, __HIP_MEMORY_SCOPE_AGENT); }
template <int NAP> DI void flag_wait(const char* ws, unsigned off, u64 tag) {
    const u64 t0 = __builtin_amdgcn_s_memrealtime();
    for (;;) {
        const u64 got = __ballot(flag_peek(ws, off) == tag);
        if (got == ~0ull) break;
        if (__builtin_amdgcn_s_memrealtime() - t0 > 2000000ull) break;
        __builtin_amdgcn_s_sleep(NAP);
    }
    asm volatile("" ::: "memory");
}
template <int NAP> DI u64 flag_wait_some(const char* ws, unsigned off, u64 tag, u64 want) {
    const u64 t0 = __builtin_amdgcn_s_memrealtime();
    u64 got;
    for (;;) {
        got = __ballot(flag_get(ws, off) == tag);
        if ((got & want) == want || __builtin_amdgcn_s_memrealtime() - t0 > 2000000ull) break;
        __builtin_amdgcn_s_sleep(NAP);
    }
    asm volatile("" ::: "memory");
    return got;
}
DI u64 next_tag(u64 e) { return e * 6364136223846793005ull + 1442695040888963407ull; }
DI u64 read_tag(const char* ws) {
    u64 e;
    asm volatile("s_load_dwordx2 %0, %1, 0x0\n\ts_waitcnt lgkmcnt(0)" : "=s"(e) : "s"(ws) : "memory");
    return next_tag(e);
}

DI int fresh_lane() { int l; asm volatile("v_mbcnt_lo_u32_b32 %0, -1, 0\n\tv_mbcnt_hi_u32_b32 %0, -1, %0" : "=v"(l)); return l; }
#define LANE16() const int lane = fresh_lane(), r16 = lane & 15, g4 = lane >> 4, q4 = r16 >> 2, p4 = lane & 3; (void)r16; (void)g4; (void)q4; (void)p4


constexpr int P_W = 49152;
constexpr int P_GW = P_W + 5 * 6144, P_E = P_GW + 1280;
constexpr int A_SLOT = 17408;
constexpr int A_Q = 4 * A_SLOT;

DI f32x16 attn_exact(rsrc_t rs, unsigned kvbase, bf16x8 qf, int lane) {
    const int r32 = lane & 31, hh = lane >> 5;
    f32x16 o, zero;
#pragma unroll
    for (int i = 0; i < 16; ++i) { o[i] = 0.f; zero[i] = 0.f; }
    float m = -INFINITY;
    const unsigned cst = ((r32 & 27) == 16) ? 0x3f803f80u : 0u;
    const u32x4 cv = {cst, cst, cst, cst};
#pragma unroll 1
    for (int t = 0; t < 64; ++t) {
        const unsigned tb = kvbase + (t >> 2) * 16384 + ((t >> 1) & 1) * 4096;
        const bf16x8 kf = BC8(ld16_dev(rs, tb + (t & 1) * 1024 + lane * 16, 0));
        const f32x16 s = MFMA32(kf, qf, zero);
        float tm = s[0];
#pragma unroll
        for (int i = 1; i < 16; ++i) tm = fmaxf(tm, s[i]);
        tm = fmaxf(tm, __shfl_xor(tm, 32));
        const float mn = fmaxf(m, tm);
        const float alpha = __builtin_amdgcn_exp2f(m - mn);
        m = mn;
#pragma unroll
        for (int i = 0; i < 16; ++i) o[i] *= alpha;
#pragma unroll
        for (int st = 0; st < 2; ++st) {
            u32x4 pw;
#pragma unroll
            for (int j = 0; j < 4; ++j) pw[j] = cvtpk(__builtin_amdgcn_exp2f(s[8 * st + 2 * j] - m), __builtin_amdgcn_exp2f(s[8 * st + 2 * j + 1] - m));
            const u32x4 vv = ld16_dev(rs, tb + 8192 + hh * 1024 + ((t & 1) * 2 + st) * 256 + (r32 & 15) * 16, 0);
            o = MFMA32(BC8(r32 < 16 ? vv : cv), BC8(pw), o);
        }
    }
    return o;
}

DI void role_a(int blk, const u16* __restrict__ x, const u16* __restrict__ wqkv, const u16* __restrict__ gw,
               const u16* __restrict__ wd, const u16* __restrict__ wo, const u16* __restrict__ wg, const u16* __restrict__ wu, char* __restrict__ ws, char* lds) {
    const int lane = fresh_lane(), wid = __builtin_amdgcn_readfirstlane(threadIdx.x >> 6);
    const int r32 = lane & 31, hh = lane >> 5;
    const int xcd = blk & 7, bi = blk >> 3;
    const int qb = (xcd & 1) * 8 + (bi & 7), hp = (xcd >> 1) * 4 + (bi >> 3);
    const unsigned lds0 = (unsigned)(uintptr_t)lds;
    LAS char* L = (LAS char*)lds;
    const rsrc_t rs = __builtin_amdgcn_make_buffer_rsrc(ws, 0, WS_END, 0x00020000);
    const unsigned kvb = WS_KV + hp * 262144;
    const unsigned kvown = kvb + qb * 16384;
    float zg = 0.f;
    __builtin_amdgcn_s_setprio(3);
#define LD8(A) (*(LAS const bf16x8*)(L + (A)))
    {
        const int lw = 2 * (wid & 3);
        const int xrow = 8 * lw + (lane >> 3);
        const unsigned xo = (unsigned)(xrow * 1024 + (((lane & 7) ^ ((xrow >> 1) & 7)) << 4)), xo2 = (xo ^ 64) + 8192;
        const unsigned wo_ = (unsigned)((4 * lw + ((lane >> 2) & 3)) * 3072 + (lane >> 4) * 1024 + (lane & 3) * 16);
        const char* xb = (const char*)x + (size_t)qb * 128 * 1024;
        const char* wb = (const char*)wqkv + hp * 64;
        auto ld_x = [&](int h) {
            const char* b = xb + (h & 1) * 65536 + (h >> 1) * 128;
            const unsigned d = lds0 + (((h >> 1) + 1) % 3) * 16384 + (h & 1) * 8192 + lw * 1024;
            dma16(xo, b, d); dma16(xo2, b, d + 1024);
        };
        auto ld_w = [&](int j) {
            const char* b = wb + (size_t)j * (32 * 3072);
            const unsigned d = lds0 + P_W + (j % 5) * 6144 + lw * 768;
            dma16_48(wo_, b, d); dma16_48(wo_, b + 4 * 3072, d + 768);
        };
        if (wid >= 4) {
#pragma unroll
            for (int j = 0; j < 5; ++j) { ld_x(j); ld_w(j); }
            ld_x(5);
        } else {
            if (wid == 3) dma16((unsigned)(lane * 16), gw, lds0 + P_GW);
            else if (wid == 2) touch((unsigned)(lane * 4), ws, lds0 + P_E);
        }
        auto warm_gu = [&](int h) {
            const int l = lane < 56 ? lane : 55, r = (l * 37) >> 8, c = l - 7 * r;
            touch((unsigned)(r * (FF * 2) + c * 128), (const char*)(wid & 1 ? wu : wg) + (size_t)(16 * bi + h * 8) * (FF * 2) + (xcd >> 1) * 896, lds0 + P_GW + 1024);
        };
        auto proj = [&](auto KIND) {
            constexpr int kind = decltype(KIND)::value, NT = kind == 0 ? 1 : 2;
            const int row0 = kind == 0 ? (wid - 4) * 32 : (wid & 1) * 64;
            int xa[4];
#pragma unroll
            for (int k = 0; k < 4; ++k) { xa[k] = ((row0 + r32) * 128 + ((hh ^ ((r32 >> 1) & 7)) << 4)) ^ (k << 5); PIN(xa[k]); }
            const int wa = P_W + hh * 1536 + kind * 256 + ((lane & 15) >> 2) * 64 + ((lane >> 4) & 1) * 32 + (lane & 3) * 8;
            int ga = (8 * hp + (lane >> 3)) * 128 + (((lane & 7) ^ ((4 * hp + (lane >> 4)) & 7)) << 4), gb = P_GW + (lane & 7) * 16;
            if (kind == 2) { PIN(ga); PIN(gb); }
            int eo = kind == 0 ? A_Q + (wid - 4) * 2048 + lane * 16
                               : (wid & 1) * 4096 + (kind == 1 ? lane * 16 : 8192 + (r32 >> 4) * 2048 + hh * 1024 + (r32 & 15) * 16);
            PIN(eo);
            f32x16 acc[NT];
#pragma unroll
            for (int t = 0; t < NT; ++t)
#pragma unroll
                for (int i = 0; i < 16; ++i) acc[t][i] = 0.f;
            bf16x8 xf[2][2][NT], wf[2][2];
            u32x4 gx, gv;
            float z = 0.f;
            static_for<0, 17>([&](auto J) {
                constexpr int j = decltype(J)::value, c = j & 1;
                constexpr int xs = (((j >> 1) + 1) % 3) * 16384, wsl = (j % 5) * 6144;
                if constexpr (j < 16) {
                    constexpr int issued = j < 2 ? 11 : (2 * j + 8 < 32 ? 2 * j + 8 : 32);
                    if (kind == 2 && j == 0 && wid == 3) WAIT_VM(0);
                    wait_vm_bar<kind == 0 ? 2 * (issued - 1 - (c ? 2 * j + 1 : 2 * j + 2)) : (j < 15 || kind == 1 ? 63 : 0)>();
                }
                auto mm = [&](int ks, int t) {
                    if constexpr (j > 0) acc[t] = kind == 2 ? MFMA32(xf[c ^ 1][ks][t], wf[c ^ 1][ks], acc[t]) : MFMA32(wf[c ^ 1][ks], xf[c ^ 1][ks][t], acc[t]);
                    SBAR();
                };
                auto rw = [&](int ks) { if constexpr (j < 16) wf[c][ks] = cat8(tr_read(L + wa + wsl + ks * 3072), tr_read(L + wa + wsl + ks * 3072 + 768)); };
                auto rx = [&](int ks, int t) { if constexpr (j < 16) xf[c][ks][t] = LD8(xa[2 * c + ks] + xs + t * 4096); };
                if constexpr (kind == 0) {
                    rw(0); rx(0, 0); rw(1); rx(1, 0);
                    SBAR();
                    if constexpr (j + 4 < 16 && j >= 2) ld_x(j + 4);
                    mm(0, 0);
                    if constexpr (j + 4 < 16 && j >= 1) ld_w(j + 4);
                    mm(1, 0);
                } else {
                    if constexpr (kind == 1 && j == 12) { warm_gu(0); warm_gu(1); }
                    mm(0, 0); rw(0); rx(0, 0); SBAR();
                    mm(0, 1); rx(0, 1); rw(1); SBAR();
                    mm(1, 0); rx(1, 0); rx(1, 1);
                    if (kind == 2 && j < 16 && c == 0 && wid == 3) { gx = *(LAS const u32x4*)(L + ga + xs); gv = *(LAS const u32x4*)(L + gb + (j >> 1) * 128); }
                    SBAR();
                    mm(1, 1);
                    if (kind == 2 && c == 1 && wid == 3) {
#pragma unroll
                        for (int e = 0; e < 4; ++e) {
                            z = fmaf(__uint_as_float(gx[e] << 16), __uint_as_float(gv[e] << 16), z);
                            z = fmaf(__uint_as_float(gx[e] & 0xffff0000u), __uint_as_float(gv[e] & 0xffff0000u), z);
                        }
                    }
                }
            });
#pragma unroll
            for (int i = 0; i < NT; ++i)
#pragma unroll
                for (int e = 0; e < 2; ++e) {
                    u32x4 v;
#pragma unroll
                    for (int j = 0; j < 4; ++j)
                        v[j] = kind == 0 ? cvtpk(acc[i][8 * e + 2 * j] * QSCALE, acc[i][8 * e + 2 * j + 1] * QSCALE) : cvtpk(acc[i][8 * e + 2 * j], acc[i][8 * e + 2 * j + 1]);
                    const int off = eo + (kind == 0 ? e * 1024 : (kind == 1 ? e * 2048 + i * 1024 : (2 * i + e) * 256));
                    if constexpr (kind > 0) __builtin_amdgcn_raw_buffer_store_b128(v, rs, off, kvown, 16);
                    *(LAS u32x4*)(L + off) = v;
                }
            if (kind == 2) zg = z;
        };
        if (wid >= 4) proj(IC<0>{}); else if (wid < 2) proj(IC<1>{}); else proj(IC<2>{});
    }
    const unsigned cst = ((lane & 11) == 0) ? 0x3f803f80u : 0u;
    if (wid == 4) *(LAS u32x4*)(L + 16384 + lane * 16) = (u32x4){cst, cst, cst, cst};
    LGKM_BAR();
    const u32x2 ew = *(LAS const u32x2*)(L + P_E);
    const u64 tag = next_tag((u64)(unsigned)__builtin_amdgcn_readfirstlane(ew[1]) << 32 | (unsigned)__builtin_amdgcn_readfirstlane(ew[0]));
    const bool mover = wid >= 4;
    const char* kvg = ws + kvb + (wid & 3) * 4096;
    const unsigned doff = (unsigned)(lane * 16);
    auto fetch = [&](int c, int slot) {
        dma64_dev(doff, kvg + (size_t)((qb + c) & 15) * 16384, lds0 + slot * A_SLOT + (wid & 3) * 4096);
    };
    if (wid > 4) *(LAS u32x4*)(L + (wid - 4) * A_SLOT + 16384 + lane * 16) = (u32x4){cst, cst, cst, cst};
    const int hsel = wid & 1, qp = (wid >> 1) & 1, kh = wid >> 2;
    const bf16x8 qA = *(LAS const bf16x8*)(L + A_Q + ((2 * qp) * 2 + hsel) * 1024 + lane * 16);
    const bf16x8 qB = *(LAS const bf16x8*)(L + A_Q + ((2 * qp + 1) * 2 + hsel) * 1024 + lane * 16);
    int ka = kh * 4096 + hsel * 2048 + lane * 16;
    int va = r32 < 16 ? 8192 + kh * 4096 + hsel * 2048 + hh * 1024 + r32 * 16 : 16384 + (r32 & 15) * 16;
    PIN(ka); PIN(va);
    bf16x8 kf = LD8(ka);
    f32x16 negm, oA, oB, sX, sY;
    float mref;
    {
        f32x16 zero;
#pragma unroll
        for (int i = 0; i < 16; ++i) { zero[i] = 0.f; oA[i] = 0.f; oB[i] = 0.f; }
        sX = MFMA32(kf, qA, zero); sY = MFMA32(kf, qB, zero);
        float tm = fmaxf(sX[0], sY[0]);
#pragma unroll
        for (int i = 1; i < 16; ++i) tm = fmaxf(tm, fmaxf(sX[i], sY[i]));
        mref = fmaxf(tm, __shfl_xor(tm, 32));
#pragma unroll
        for (int i = 0; i < 16; ++i) negm[i] = -mref;
        PIN(negm);
    }
    sX = MFMA32(kf, qA, negm);
    bf16x8 v0 = LD8(va), v1 = v0;
    u32x4 p0, p1 = {0u, 0u, 0u, 0u};
    auto publish = [&]() {
        WAIT_VM(0);
        if (fresh_lane() == 0) flag_set(ws, WS_KVF + hp * 512 + (unsigned)(wid * 16 + qb) * 8, tag);
    };
    if (wid < 4) publish();
    if (wid == 3) {
        zg += __shfl_xor(zg, 1); zg += __shfl_xor(zg, 2); zg += __shfl_xor(zg, 4);
        if ((lane & 7) == 0) __builtin_amdgcn_raw_buffer_store_b32(__float_as_uint(1.f / (1.f + __expf(-zg))), rs, WS_G + (unsigned)(qb * 128 + 8 * hp + (lane >> 3)) * 4, 0, 16);
    }
#define EX2(X, i) X[i] = __builtin_amdgcn_exp2f(X[i])
#define HALF(SC, M1, L1, M2, L2, M3, L3) do { \
        EX2(SC, 0); EX2(SC, 1); PIN(SC); SBAR(); \
        M1; SBAR(); L1; \
        EX2(SC, 2); EX2(SC, 3); EX2(SC, 4); EX2(SC, 5); EX2(SC, 6); EX2(SC, 7); PIN(SC); SBAR(); \
        M2; SBAR(); L2; \
        p0[0] = cvtpk(SC[0], SC[1]); p0[1] = cvtpk(SC[2], SC[3]); p0[2] = cvtpk(SC[4], SC[5]); p0[3] = cvtpk(SC[6], SC[7]); PIN(p0); SBAR(); \
        EX2(SC, 8); EX2(SC, 9); EX2(SC, 10); EX2(SC, 11); PIN(SC); SBAR(); \
        M3; SBAR(); L3; \
        EX2(SC, 12); EX2(SC, 13); EX2(SC, 14); EX2(SC, 15); \
        p1[0] = cvtpk(SC[8], SC[9]); p1[1] = cvtpk(SC[10], SC[11]); p1[2] = cvtpk(SC[12], SC[13]); p1[3] = cvtpk(SC[14], SC[15]); PIN(p1); SBAR(); \
    } while (0)
#define PR(V) do { if ((V) >= 0) __builtin_amdgcn_s_setprio((V) < 0 ? 0 : (V)); } while (0)
#define CHUNK(S0, S1, SYNC, Q1, Q3, Q4) do { \
        PR(Q1); \
        HALF(sX, oB = MFMA32(v1, BC8(p1), oB), v1 = LD8(va + (S0) + 256), sY = MFMA32(kf, qB, negm), kf = LD8(ka + (S0) + 1024), oA = MFMA32(v0, BC8(p0), oA), (void)0); \
        HALF(sY, oA = MFMA32(v1, BC8(p1), oA), (void)0, sX = MFMA32(kf, qA, negm), (void)0, oB = MFMA32(v0, BC8(p0), oB), v0 = LD8(va + (S0) + 512)); \
        PR(Q3); \
        HALF(sX, oB = MFMA32(v1, BC8(p1), oB), v1 = LD8(va + (S0) + 768), sY = MFMA32(kf, qB, negm), SYNC; kf = LD8(ka + (S1)), oA = MFMA32(v0, BC8(p0), oA), (void)0); \
        PR(Q4); \
        HALF(sY, oA = MFMA32(v1, BC8(p1), oA), (void)0, sX = MFMA32(kf, qA, negm), (void)0, oB = MFMA32(v0, BC8(p0), oB), v0 = LD8(va + (S1))); \
    } while (0)
    auto warm = [&](int q) {
        const int idx = bi * 2 + (wid & 3);
        if ((wid & 3) < 2 && q >= 2 && q <= 6 && (q == 6 || idx < 56))
            touch(doff * 8, (q == 6 ? (const char*)wo : (const char*)wd + (q - 2) * 458752) + idx * 8192, lds0 + A_Q + wid * 256);
    };
    auto late = [&](int q) {
        if (q == 1) {
#pragma unroll
            for (int e = 0; e < 2; ++e)
                __builtin_amdgcn_raw_buffer_store_b128((u32x4){EMPTY, EMPTY, EMPTY, EMPTY}, rs, doff, WS_O + (unsigned)((4 * qb + (wid & 3)) * 32 + 2 * hp + e) * 1024, 16);
        }
        if (q == 2 && (wid & 3) == 3 && fresh_lane() == 0) flag_set(ws, WS_PF + qb * 128 + hp * 8, tag);
    };
#pragma unroll
    for (int n = 0; n < 4; ++n) {
        auto sync_a = [&]() {
            if (n == 0) {
                if (wid == 0) flag_wait_some<3>(ws, WS_KVF + hp * 512 + (unsigned)fresh_lane() * 8, tag, ~0ull);
                BAR();
#pragma unroll
                for (int c = 1; c < 3; ++c) dma32_dev(doff, ws + kvb + ((qb + c) & 15) * 16384 + wid * 2048, lds0 + c * A_SLOT + wid * 2048);
                __builtin_amdgcn_s_setprio(0);
            }
            if (n == 0) WAIT_VM_BAR(2); else WAIT_VM_BAR(0);
            PR(3);
            if (mover) { fetch(4 * n + 3, 3); if (n < 3) fetch(4 * n + 4, 0); }
            else { warm(2 * n); late(2 * n); }
        };
        auto sync_b = [&]() {
            WAIT_VM_BAR(0);
            PR(3);
            if (mover) { if (n < 3) { fetch(4 * n + 5, 1); fetch(4 * n + 6, 2); } }
            else { warm(2 * n + 1); late(2 * n + 1); }
        };
        if (n == 0) {
            HALF(sX, oB = MFMA32(v1, BC8(p1), oB), v1 = LD8(va + 256), sY = MFMA32(kf, qB, negm), kf = LD8(ka + 1024), oA = MFMA32(v0, BC8(p0), oA), (void)0);
            HALF(sY, oA = MFMA32(v1, BC8(p1), oA), (void)0, sX = MFMA32(kf, qA, negm), (void)0, oB = MFMA32(v0, BC8(p0), oB), v0 = LD8(va + 512));
            HALF(sX, oB = MFMA32(v1, BC8(p1), oB), v1 = LD8(va + 768), sY = MFMA32(kf, qB, negm), (void)0, oA = MFMA32(v0, BC8(p0), oA), (void)0);
            HALF(sY, oA = MFMA32(v1, BC8(p1), oA), (void)0, (void)0, (void)0, oB = MFMA32(v0, BC8(p0), oB), (void)0);
            sync_a();
            kf = LD8(ka + A_SLOT); v0 = LD8(va + A_SLOT);
            sX = MFMA32(kf, qA, negm);
            SBAR();
        } else CHUNK(0, A_SLOT, sync_a(), 0, -1, -1);
        auto sync_1 = [&]() { if (n == 0) { if (!mover) WAIT_VM(0); WAIT_VM_BAR(8); } };
        CHUNK(A_SLOT, 2 * A_SLOT, sync_1(), 2, 1, -1);
        CHUNK(2 * A_SLOT, 3 * A_SLOT, sync_b(), 0, -1, -1);
        if (n < 3) CHUNK(3 * A_SLOT, 0, (void)0, 2, 1, -1); else CHUNK(3 * A_SLOT, 0, (void)0, 2, 1, 0);
    }
    oB = MFMA32(v1, BC8(p1), oB);
    auto finish = [&](const f32x16& give, const f32x16& keep, bf16x8 qf) {
        LAS char* ew = L + A_SLOT + wid * 3072 + doff;
        LAS const char* er = L + A_SLOT + (wid ^ 4) * 3072 + doff;
        *(LAS f32x4*)ew = (f32x4){give[0], give[1], give[2], give[3]};
        *(LAS f32x4*)(ew + 1024) = (f32x4){give[4], give[5], give[6], give[7]};
        *(LAS f32x2*)(ew + 2048) = (f32x2){give[8], mref};
        LGKM_BAR();
        const f32x2 e2 = *(LAS const f32x2*)(er + 2048);
        const f32x4 e0 = *(LAS const f32x4*)er, e1 = *(LAS const f32x4*)(er + 1024);
        const float mm = fmaxf(mref, e2[1]), f1 = __builtin_amdgcn_exp2f(mref - mm), f2 = __builtin_amdgcn_exp2f(e2[1] - mm);
        const float l = keep[8] * f1 + e2[0] * f2, inv = __builtin_amdgcn_rcpf(l), g1 = f1 * inv, g2 = f2 * inv;
        f32x16 o;
#pragma unroll
        for (int i = 0; i < 8; ++i) o[i] = keep[i] * g1 + (i < 4 ? e0[i & 3] : e1[i & 3]) * g2;
        unsigned big = 0;
#pragma unroll
        for (int i = 0; i < 8; ++i) big = max(big, __float_as_uint(o[i]) << 1);
        const bool ok = keep[8] < 1.0995116e12f && e2[0] < 1.0995116e12f && l > 9.0949470e-13f && big < 0xff000000u;
        if (__builtin_expect(!__all(ok), 0)) { o = attn_exact(rs, kvb + hsel * 2048, qf, lane); o *= 1.f / o[8]; }
        u32x4 w;
#pragma unroll
        for (int j = 0; j < 4; ++j) w[j] = min(cvtpk(o[2 * j], o[2 * j + 1]), EMPTY - 1);
        __builtin_amdgcn_raw_buffer_store_b128(w, rs, doff, WS_O + (unsigned)((4 * qb + 2 * qp + kh) * 32 + 2 * hp + hsel) * 1024, 16);
    };
    if (kh) finish(oA, oB, qB); else finish(oB, oA, qA);
    if (blk == 0 && wid == 7) {
        const u64 te = __builtin_amdgcn_s_memrealtime();
        for (;;) {
            bool ok = true;
#pragma unroll
            for (int i = 0; i < 4; ++i) {
                const int w = i * 64 + lane;
                ok = ok & (flag_peek(ws, WS_KVF + (w >> 4) * 512 + (w & 15) * 8) == tag) & (flag_peek(ws, WS_HF + (w >> 4) * 1024 + (w & 15) * 32) == tag);
            }
            if (__all(ok) || __builtin_amdgcn_s_memrealtime() - te > 2000000ull) break;
            __builtin_amdgcn_s_sleep(8);
        }
        if (lane == 0) flag_set(ws, WS_E, tag);
    }
}

#define F_HI() __builtin_amdgcn_s_setprio(3)
#define F_LO() __builtin_amdgcn_s_setprio(0)
DI void role_f(int blk, const u16* __restrict__ x, const u16* __restrict__ wqkv, const u16* __restrict__ wo, const u16* __restrict__ wg, const u16* __restrict__ wu,
               const u16* __restrict__ wd, char* __restrict__ ws, u16* __restrict__ out, char* lds) {
    const int wid = __builtin_amdgcn_readfirstlane(threadIdx.x >> 6);
    const int xcd = blk & 7, bi = blk >> 3;
    const int cq = xcd >> 1, rb = (xcd & 1) * 8 + (bi & 7), cb = cq * 4 + (bi >> 3);
    const int mb = 4 * xcd + (bi >> 3), nb = bi & 7;
    const unsigned lds0 = (unsigned)(uintptr_t)lds;
    LAS char* L = (LAS char*)lds;
    const rsrc_t rs = __builtin_amdgcn_make_buffer_rsrc(ws, 0, WS_END, 0x00020000);
    const u64 tstart = __builtin_amdgcn_s_memrealtime();
    auto wait_until = [&](unsigned ticks) { while ((unsigned)(__builtin_amdgcn_s_memrealtime() - tstart) < ticks) __builtin_amdgcn_s_sleep(2); };
    F_HI();
    {
        const int lane = fresh_lane(), idx = bi * 8 + wid;
        __builtin_amdgcn_s_sleep(4);
        const int xl = lane < 20 ? lane : 19;
        touch((unsigned)(idx * 4096 + (xl & 3) * 1024 + (3 + (xl >> 2)) * 128), (const char*)x + (size_t)(xcd & 1) * (1 << 20), lds0 + wid * 1024);
        const int q = lane < 12 ? lane : 11, qr = q >= 6, qc = q - 6 * qr;
        if (idx >= 80) touch((unsigned)((2 * idx + qr) * 3072 + (qc >> 1) * 1024 + (qc & 1) * 128), (const char*)wqkv + cq * 256, lds0 + wid * 1024);
    }
    u64 tag;
    {
        LANE16();
        const int r32 = lane & 31, hh = lane >> 5;
        const int xrow = 8 * wid + (lane >> 3);
        unsigned xo = (unsigned)(xrow * 1024 + (((lane & 7) ^ ((xrow >> 1) & 7)) << 4));
        const int bg = (r16 >> 1) ^ (2 * g4), bu = bg ^ 1;
        const unsigned wrow = (unsigned)((4 * wid + g4) * (FF * 2) + (lane & 1) * 16);
        const int odd = cb & 1;
        unsigned go = wrow + bg * 32, uo = wrow + bu * 32;
        PIN(xo); PIN(go); PIN(uo);
        const char* xb = (const char*)x + (size_t)rb * 128 * 1024;
        const char* gb = (const char*)wg + cb * 224 - odd * 32;
        const char* ub = (const char*)wu + cb * 224 - odd * 32;
        const unsigned dw = lds0 + wid * 1024;
        auto ld_x = [&](int s) { const unsigned d = dw + (s & 1) * 16384; dma16(xo, xb + s * 128, d); dma16(xo, xb + 65536 + s * 128, d + 8192); };
        auto ld_w = [&](int j) {
            const unsigned d = dw + 32768 + (j % 3) * 16384; const size_t o = (size_t)j * (32 * FF * 2); dma16(go, gb + o, d); dma16(uo, ub + o, d + 8192); };
        auto gu = [&](auto KIND) {
            constexpr int NT = decltype(KIND)::value, NU = 4 - NT;
            const int t0 = NT == 2 ? (wid & 1) * 2 : wid - 4, u0 = NT == 2 ? (wid >> 1) * 2 : 4;
            LAS const char *xa[4], *wa[NU];
#pragma unroll
            for (int k = 0; k < 4; ++k) { xa[k] = L + (((t0 * 32 + r32) * 128 + ((hh ^ ((r32 >> 1) & 7)) << 4)) ^ (k << 5)); PIN(xa[k]); }
#pragma unroll
            for (int u = 0; u < NU; ++u) { wa[u] = L + 32768 + (g4 & 1) * 8192 + (8 * hh + q4) * 256 + (((u0 + u + odd) ^ (2 * q4 + (g4 & 1))) << 5) + p4 * 8; PIN(wa[u]); }
            f32x16 acc[NT][NU];
#pragma unroll
            for (int t = 0; t < NT; ++t)
#pragma unroll
                for (int u = 0; u < NU; ++u)
#pragma unroll
                    for (int i = 0; i < 16; ++i) acc[t][u][i] = 0.f;
            bf16x8 x0[NT], w0[NU], x1[2][NT], w1[2][NU];
            auto rd = [&](auto J, int ks, bf16x8 (&xf)[NT], bf16x8 (&wf)[NU]) {
                constexpr int j = decltype(J)::value;
#pragma unroll
                for (int t = 0; t < NT; ++t) xf[t] = *(LAS const bf16x8*)(xa[2 * (j & 1) + ks] + ((j >> 1) & 1) * 16384 + t * 4096);
#pragma unroll
                for (int u = 0; u < NU; ++u) { LAS const char* a = wa[u] + (j % 3) * 16384 + ks * 4096; wf[u] = cat8(tr_read(a), tr_read(a + 1024)); }
            };
            auto mm = [&](bf16x8 (&xf)[NT], bf16x8 (&wf)[NU]) {
#pragma unroll
                for (int t = 0; t < NT; ++t)
#pragma unroll
                    for (int u = 0; u < NU; ++u) acc[t][u] = MFMA32(wf[u], xf[t], acc[t][u]);
            };
            wait_until(300);
            tag = read_tag(ws);
            wait_until(600);
            ld_x(0); ld_w(0);
            BAR();
            ld_w(1); ld_x(1); ld_w(2);
            F_LO();
            WAIT_VM_BAR(6);
            rd(IC<0>{}, 0, x0, w0); rd(IC<0>{}, 1, x1[0], w1[0]);
            static_for<0, 16>([&](auto J) {
                constexpr int j = decltype(J)::value, c = j & 1;
                wait_vm_bar<j >= 14 ? 0 : (c ? 2 : 4)>();
                if constexpr (c == 0) { if constexpr (j + 3 < 16) ld_w(j + 3); }
                else if constexpr (j + 3 < 16) { ld_x((j + 3) >> 1); ld_w(j + 3); }
                if constexpr (j < 15) rd(IC<j + 1>{}, 1, x1[c ^ 1], w1[c ^ 1]);
                SBAR();
                mm(x0, w0);
                SBAR();
                if constexpr (j < 15) rd(IC<j + 1>{}, 0, x0, w0);
                SBAR();
                mm(x1[c], w1[c]);
                SBAR();
            });
            F_HI();
            const int lane = fresh_lane();
#pragma unroll
            for (int t = 0; t < NT; ++t)
#pragma unroll
                for (int u = 0; u < NU; ++u) {
                    u32x4 v;
#pragma unroll
                    for (int j = 0; j < 4; ++j) v[j] = cvtpk(acc[t][u][2 * j] * acc[t][u][8 + 2 * j], acc[t][u][2 * j + 1] * acc[t][u][9 + 2 * j]);
                    st16_dev(rs, WS_H + (unsigned)((rb * 4 + t0 + t) * 112 + 7 * cb + u0 + u) * 1024 + lane * 16, v);
                }
            WAIT_VM(0);
            if (lane == 0) flag_set(ws, WS_HF + (2 * rb + (t0 >> 1)) * 512 + (unsigned)(cb * 4 + (NT == 2 ? wid >> 1 : 2 + (wid & 1))) * 8, tag);
        };
        if (wid < 4) gu(IC<2>{}); else gu(IC<1>{});
    }
#define WDO() (unsigned)((lane >> 3) * 1024 + ((((lane & 7) >> 1) ^ (((lane >> 3) & 2) | (lane >> 5))) << 5) + (lane & 1) * 16)
    {
        LANE16();
        const int r32 = lane & 31, hh = lane >> 5;
        const unsigned wdo = WDO(), hv = (unsigned)(lane * 16);
        const char* wdb = (const char*)wd + (size_t)wid * 8192 + nb * 128;
        const char* hb = ws + WS_H + (size_t)((2 * mb + (wid >> 2)) * 112 + (wid & 3)) * 1024;
        unsigned dd = lds0 + wid * 1024;
        asm volatile("" : "+s"(dd));
        auto ld_wd = [&](int s) {
            dma16(wdo, wdb + (size_t)s * 65536, dd + (s % 5) * 16384 + 8192); };
        auto ld_h = [&](int s) { dma16_dev(hv, hb + s * 4096, dd + (s % 5) * 16384); };
#pragma unroll
        for (int s = 0; s < 4; ++s) ld_wd(s);
        const int t = wid & 1, c = (wid >> 1) & 1, kh = wid >> 2;
        LAS const char *ha[2], *wa[2];
        LAS f32x4* red = (LAS f32x4*)(L + 32768 + ((c * 8 + hh) * 64 + t * 32 + r32) * 16);
        PIN(red);
#pragma unroll
        for (int e = 0; e < 2; ++e) {
            ha[e] = L + e * 49152 + (t * 4 + 2 * kh) * 1024 + lane * 16;
            wa[e] = L + e * 49152 + 8192 + kh * 4096 + (4 * hh + q4) * 128 + (((2 * c + (g4 & 1)) ^ ((q4 & 2) | hh)) << 5) + p4 * 8;
            PIN(ha[e]); PIN(wa[e]);
        }
        unsigned hfo = WS_HF + mb * 512 + lane * 8;
        LAS const char* rd = wa[0] - 8192 - kh * 4096 + (kh ? 0 : 49152);
        unsigned oo = (unsigned)((mb * 64 + t * 32 + r32) * DM + nb * 64 + c * 32 + (2 * kh + hh) * 8) * 2;
        PIN(hfo); PIN(rd); PIN(oo);
        u32x4 of[16];
        const unsigned os = WS_O + (unsigned)((2 * mb + t) * 32 + 16 * kh) * 1024;
        u64 seen = ~0ull;
        if (wid == 0) seen = flag_wait_some<4>(ws, hfo, tag, 0xffffull);
        if (wid == 1) flag_wait<4>(ws, WS_PF + (mb >> 1) * 128 + r16 * 8, tag);
        const char* wob = (const char*)wo + (size_t)wid * 32768 + nb * 128;
        auto ld_wo = [&](int half) {
#pragma unroll
            for (int e = 0; e < 4; ++e) dma16(wdo, wob + half * 262144 + e * 8192, lds0 + (half ? 0 : 49152) + wid * 4096 + e * 1024);
        };
        BAR();
        const unsigned gbits = __builtin_amdgcn_raw_buffer_load_b32(rs, WS_G + (unsigned)(mb * 64 + t * 32 + r32) * 4, 0, 16);
#pragma unroll
        for (int s = 0; s < 4; ++s) ld_h(s);
        F_LO();
        f32x16 acc;
#pragma unroll
        for (int i = 0; i < 16; ++i) acc[i] = 0.f;
        bf16x8 hf[2][2], wf[2][2];
        static_for<0, 29>([&](auto S) {
            constexpr int s = decltype(S)::value, p = s & 1, sl = s % 5, e = sl >= 3, so = (sl - 3 * e) * 16384;
            if constexpr (s < 28) {
                if constexpr (s % 7 == 3 && s < 24) {
                    constexpr u64 want = 0xffffull << (16 * ((s + 4) / 7));
                    unsigned part = (unsigned)(seen >> (16 * ((s + 4) / 7)));
                    asm volatile("" : "+s"(part));
                    if ((part & 0xffffu) != 0xffffu) { F_HI(); seen = flag_wait_some<4>(ws, hfo, tag, want); F_LO(); }
                }
                wait_vm_bar<(s < 4 ? 3 + s : (s < 25 ? 6 : (s == 25 ? 4 : 58 - 2 * s)))>();
                if constexpr (s + 4 < 28) { ld_wd(s + 4); ld_h(s + 4); }
                if constexpr (s == 25) ld_wo(0);
                if constexpr (s == 27) ld_wo(1);
#pragma unroll
                for (int k = 0; k < 16; ++k) if (s == 27) of[k] = ld16_dev(rs, hv, os + k * 1024);
#pragma unroll
                for (int k = 0; k < 2; ++k) {
                    hf[p][k] = *(LAS const bf16x8*)(ha[e] + so + k * 1024);
                    wf[p][k] = cat8(tr_read(wa[e] + so + k * 2048), tr_read(wa[e] + so + k * 2048 + 1024));
                }
            }
            SBAR();
            if constexpr (s == 28) F_HI();
            if constexpr (s > 1 && s < 28) PIN(acc);
            if constexpr (s > 0) { acc = MFMA32(wf[p ^ 1][0], hf[p ^ 1][0], acc); acc = MFMA32(wf[p ^ 1][1], hf[p ^ 1][1], acc); }
            SBAR();
        });
        const float gt = __uint_as_float(gbits);
        acc *= 1.f - gt;
        PIN(acc);
        WAIT_VM_BAR(16);
        constexpr int WFD = 4;
        bf16x8 wq[WFD];
        auto rdw = [&](int k) { wq[k % WFD] = cat8(tr_read(rd + k * 2048), tr_read(rd + k * 2048 + 1024)); };
#pragma unroll
        for (int k = 0; k < WFD; ++k) rdw(k);
        SBAR();
        unsigned need = 0xffff;
        auto check = [&](unsigned which) {
#pragma unroll
            for (int k = 0; k < 16; ++k)
                if (which >> k & 1) { if (!__any(max(max(of[k][0], of[k][1]), max(of[k][2], of[k][3])) == EMPTY)) need &= ~(1u << k); }
        };
        {
            unsigned m = 0;
#pragma unroll
            for (int k = 0; k < 16; ++k) m = max(max(max(max(m, of[k][0]), of[k][1]), of[k][2]), of[k][3]);
            PIN(m);
            if (__builtin_expect(__any(m == EMPTY), 0)) check(0xffff); else need = 0;
        }
        SBAR();
#pragma unroll
        for (int k = 0; k < WFD; ++k) PIN(wq[k]);
        if (need) {
            const u64 tw = __builtin_amdgcn_s_memrealtime();
            F_LO();
#define OLD(K, R) "s_bitcmp1_b32 %10, " #K "\n\ts_cbranch_scc0 1f\n\ts_add_u32 %9, %13, " #K "*1024\n\tbuffer_load_dwordx4 " R ", %11, %12, %9 offen sc1\n1:\n\t"
            const unsigned pv_ = hv * 64;
            unsigned pend = 0;
            do {
                unsigned so, v = EMPTY;
                if (__builtin_popcount(need) <= 8) pend = need;
                asm volatile("s_cmp_lg_u32 %10, 0\n\ts_cbranch_scc1 1f\n\ts_sleep %16\n1:\n\t"
                             OLD(0, "%0") OLD(1, "%1") OLD(2, "%2") OLD(3, "%3") OLD(4, "%4") OLD(5, "%5") OLD(6, "%6") OLD(7, "%7")
                             "s_cmp_eq_u32 %14, 0\n\ts_cbranch_scc1 1f\n\ts_mov_b32 exec_lo, %14\n\ts_mov_b32 exec_hi, 0\n\t"
                             "buffer_load_dword %8, %15, %12, %13 offen sc1\n\ts_mov_b64 exec, -1\n1:"
                             : "+v"(of[0]), "+v"(of[1]), "+v"(of[2]), "+v"(of[3]), "+v"(of[4]), "+v"(of[5]), "+v"(of[6]), "+v"(of[7]), "+v"(v), "=&s"(so)
                             : "s"(pend), "v"(hv), "s"(rs), "s"(os), "s"(need & ~pend), "v"(pv_), "n"(4) : "memory", "scc");
                asm volatile(OLD(8, "%0") OLD(9, "%1") OLD(10, "%2") OLD(11, "%3") OLD(12, "%4") OLD(13, "%5") OLD(14, "%6") OLD(15, "%7") "s_waitcnt vmcnt(0)"
                             : "+v"(of[8]), "+v"(of[9]), "+v"(of[10]), "+v"(of[11]), "+v"(of[12]), "+v"(of[13]), "+v"(of[14]), "+v"(of[15]), "+v"(v), "=&s"(so)
                             : "s"(pend), "v"(hv), "s"(rs), "s"(os) : "memory", "scc");
#pragma unroll
                for (int k = 0; k < 8; ++k) PIN(of[k]);
                check(pend);
                pend = (pend | (unsigned)__ballot(v != EMPTY)) & need;
            } while (need && __builtin_amdgcn_s_memrealtime() - tw < 2000000ull);
        }
        f32x16 ao;
#pragma unroll
        for (int i = 0; i < 16; ++i) ao[i] = 0.f;
#pragma unroll
        for (int k = 0; k < 16; ++k) {
            ao = MFMA32(wq[k % WFD], BC8(of[k]), ao);
            SBAR();
            if (k + WFD < 16) rdw(k + WFD);
            SBAR();
        }
        acc += ao * gt;
        auto fin = [&](auto KH) {
            constexpr int a = 2 * decltype(KH)::value, b = 2 - a;
#pragma unroll
            for (int j = b; j < b + 2; ++j) red[j * 128] = (f32x4){acc[4 * j], acc[4 * j + 1], acc[4 * j + 2], acc[4 * j + 3]};
            LGKM_BAR();
            const f32x4 r0 = red[a * 128] + (f32x4){acc[4 * a], acc[4 * a + 1], acc[4 * a + 2], acc[4 * a + 3]};
            const f32x4 r1 = red[a * 128 + 128] + (f32x4){acc[4 * a + 4], acc[4 * a + 5], acc[4 * a + 6], acc[4 * a + 7]};
            const auto s0 = __builtin_amdgcn_permlane32_swap(cvtpk(r0[0], r0[1]), cvtpk(r1[0], r1[1]), false, false);
            const auto s1 = __builtin_amdgcn_permlane32_swap(cvtpk(r0[2], r0[3]), cvtpk(r1[2], r1[3]), false, false);
            *(u32x4*)((char*)out + oo) = (u32x4){s0[0], s1[0], s0[1], s1[1]};
        };
        if (kh) fin(IC<1>{}); else fin(IC<0>{});
    }
}

__global__ __launch_bounds__(512, 4) void fused_fwd(const u16* __restrict__ x, const u16* __restrict__ wqkv, const u16* __restrict__ wo,
                                                     const u16* __restrict__ wg, const u16* __restrict__ wu, const u16* __restrict__ wd,
                                                     const u16* __restrict__ gw, char* __restrict__ ws, u16* __restrict__ out) {
    extern __shared__ __attribute__((aligned(16))) char lds[];
    const char* code;
    asm volatile("s_getpc_b64 %0" : "=s"(code));
    if (blockIdx.x < 24 && threadIdx.x < 128) {
        const unsigned o = ((blockIdx.x >> 3) * 128 + threadIdx.x) * 128;
        touch(min(o, CODE_BYTES), code, (unsigned)(uintptr_t)lds + P_GW + 1024);
    }
    if (__builtin_expect(blockIdx.x >= 256, 0)) role_f(blockIdx.x - 256, x, wqkv, wo, wg, wu, wd, ws, out, lds);
    else role_a(blockIdx.x, x, wqkv, gw, wd, wo, wg, wu, ws, lds);
}

extern "C" void kernel_launch(void* const* d_in, const int* in_sizes, int n_in, void* d_out, int out_size, void* d_ws, size_t ws_size,
                              hipStream_t stream) {
    (void)hipFuncSetAttribute((const void*)fused_fwd, hipFuncAttributeMaxDynamicSharedMemorySize, LDS_BYTES);
    fused_fwd<<<512, 512, LDS_BYTES, stream>>>((const u16*)d_in[0], (const u16*)d_in[1], (const u16*)d_in[2], (const u16*)d_in[4],
                                               (const u16*)d_in[5], (const u16*)d_in[6], (const u16*)d_in[7], (char*)d_ws, (u16*)d_out);
}
```
